```python
import torch

def _softmax(x, axis=-1):
    return torch.softmax(x, dim=axis)
def _gelu(x):
    return torch.nn.functional.gelu(x, approximate="tanh")
def _silu(x):
    return torch.nn.functional.silu(x)
def _sigmoid(x):
    return torch.sigmoid(x)
def _layernorm(x, axis=-1, eps=1e-5):
    m = x.mean(dim=axis, keepdim=True)
    v = x.var(dim=axis, keepdim=True, unbiased=False)
    return (x - m) / torch.sqrt(v + eps)
def _scatter_add(tgt, idx, src, axis=0):
    return tgt.clone().index_add_(axis, idx, src)
def _segment_sum(src, seg, n):
    out = torch.zeros((n,) + src.shape[1:], dtype=src.dtype)
    return out.index_add_(0, seg, src)
def _pad(x, pw):
    flat = tuple(v for pair in reversed(pw) for v in pair)
    return torch.nn.functional.pad(x, flat)

kernel_name = 'recurrent_scan__mha_block__glu_gated_glu__prompt'

def setup_inputs():

    _w = (lambda a: (lambda t: t.to(torch.bfloat16) if t.is_floating_point() else t)(torch.as_tensor(a)))
    def _A_setup():
        B, S, D, H = 1, 2048, 512, 32
        return {
            "x": _w((lambda *s: torch.randn(*s, dtype=torch.bfloat16) * 0.05)(B, S, D)),
            "wqkv": _w((lambda *s: torch.randn(*s, dtype=torch.bfloat16) * 0.05)(D, 3 * D)),
            "wo": _w((lambda *s: torch.randn(*s, dtype=torch.bfloat16) * 0.05)(D, D)),
        }
    _A_in = _A_setup()

    def _B_setup():
        B, S, D, F = 1, 2048, 512, 1792
        return {
            "x": _w((lambda *s: torch.randn(*s, dtype=torch.bfloat16) * 0.05)(B, S, D)),
            "w_gate": _w((lambda *s: torch.randn(*s, dtype=torch.bfloat16) * 0.05)(D, F)),
            "w_up": _w((lambda *s: torch.randn(*s, dtype=torch.bfloat16) * 0.05)(D, F)),
            "w_down": _w((lambda *s: torch.randn(*s, dtype=torch.bfloat16) * 0.05)(F, D)),
        }
    _B_in = _B_setup()

    return {
        "A_x": _A_in["x"],
        "A_wqkv": _A_in["wqkv"],
        "A_wo": _A_in["wo"],
        "B_x": _B_in["x"],
        "B_w_gate": _B_in["w_gate"],
        "B_w_up": _B_in["w_up"],
        "B_w_down": _B_in["w_down"],
    }

def reference(A_x, A_wqkv, A_wo, B_x, B_w_gate, B_w_up, B_w_down):
    def _A_ref(x, wqkv, wo):
        B, S, D = x.shape
        H = 32
        Dh = D // H
        qkv = (lambda x, *d: x.permute(*d))((x @ wqkv).reshape(B, S, 3, H, Dh), 2, 0, 3, 1, 4)
        q, k, v = qkv[0], qkv[1], qkv[2]
        scores = torch.einsum("bhqd,bhkd->bhqk", q, k) * (Dh ** -0.5)
        p = _softmax(scores, axis=-1)
        o = torch.einsum("bhqk,bhkd->bhqd", p, v)
        return (lambda x, *d: x.permute(*d))(o, 0, 2, 1, 3).reshape(B, S, D) @ wo

    def _B_ref(x, w_gate, w_up, w_down):
        g = torch.einsum("bsd,df->bsf", x, w_gate)
        u = torch.einsum("bsd,df->bsf", x, w_up)
        h = _sigmoid(g) * u
        return torch.einsum("bsf,fd->bsd", h, w_down)

    x = A_x
    xn = _layernorm(x, axis=-1)
    s = _A_ref(xn, A_wqkv, A_wo)
    g = _silu(_B_ref(xn, B_w_gate, B_w_up, B_w_down))
    return x + s * g
```

```cpp
#include <hip/hip_runtime.h>
#include <hip/hip_bf16.h>

typedef __attribute__((ext_vector_type(2))) __bf16 bf16x2;
typedef __attribute__((ext_vector_type(4))) __bf16 bf16x4;
typedef __attribute__((ext_vector_type(8))) __bf16 bf16x8;
typedef __attribute__((ext_vector_type(16))) __bf16 bf16x16;
typedef __attribute__((ext_vector_type(4))) float f32x4;
typedef __attribute__((ext_vector_type(8))) float f32x8;
typedef __attribute__((ext_vector_type(16))) float f32x16;
typedef __attribute__((ext_vector_type(2))) unsigned u32x2;
typedef __attribute__((ext_vector_type(4))) unsigned u32x4;
#define AS3 __attribute__((address_space(3)))
#define DI __device__ __forceinline__

namespace {

constexpr int SEQ = 2048, DM = 512, FF = 1792;
constexpr size_t WS_Q = 0;
constexpr size_t WS_KV = WS_Q + 2097152;
constexpr size_t WS_H = WS_KV + 4194304;
constexpr size_t WS_WD = WS_H + 64 * 112 * 1024;
constexpr size_t WS_WO = WS_WD + 1792 * 1024;
constexpr size_t WS_O = WS_WO + 512 * 1024;
constexpr size_t WS_G = WS_O + 2097152;

template <class T> DI T lds_ld(unsigned a) { return *(const AS3 T*)(a); }
template <class T> DI void lds_st(unsigned a, T v) { *(AS3 T*)(a) = v; }
DI bf16x4 lds_tr(unsigned a) { return __builtin_amdgcn_ds_read_tr16_b64_v4bf16((AS3 bf16x4*)(a)); }
DI bf16x8 cat8(bf16x4 a, bf16x4 b) { return __builtin_shufflevector(a, b, 0, 1, 2, 3, 4, 5, 6, 7); }
DI f32x16 mfma(bf16x8 a, bf16x8 b, f32x16 c) { return __builtin_amdgcn_mfma_f32_32x32x16_bf16(a, b, c, 0, 0, 0); }
DI f32x16 zero16() { f32x16 z; for (int i = 0; i < 16; ++i) z[i] = 0.f; return z; }
DI void glds16(const void* gsrc, unsigned lds_dst) {
#if defined(__HIP_DEVICE_COMPILE__)
  asm volatile("s_mov_b32 m0, %1\n\ts_nop 0\n\tglobal_load_lds_dwordx4 %0, off" ::"v"(gsrc), "s"(lds_dst) : "memory");
#endif
}
template <int N> DI void wait_vm_barrier() {
  asm volatile("s_waitcnt vmcnt(%0) lgkmcnt(0)\n\ts_barrier" ::"i"(N) : "memory");
}
template <int N> DI void wait_vm() { asm volatile("s_waitcnt vmcnt(%0)" ::"i"(N) : "memory"); }
DI float dot2(bf16x2 a, bf16x2 b, float c) { return __builtin_amdgcn_fdot2_f32_bf16(a, b, c, false); }
DI bf16x2 pair(bf16x8 v, int i) { bf16x2 r = {v[2 * i], v[2 * i + 1]}; return r; }
DI float bfround(float x) { return (float)(__bf16)x; }
DI float xhalf_sum(float v) {
  auto r = __builtin_amdgcn_permlane32_swap(__float_as_uint(v), __float_as_uint(v), false, false);
  return __uint_as_float(r[0]) + __uint_as_float(r[1]);
}
DI float xhalf_max(float v) {
  auto r = __builtin_amdgcn_permlane32_swap(__float_as_uint(v), __float_as_uint(v), false, false);
  return fmaxf(__uint_as_float(r[0]), __uint_as_float(r[1]));
}
DI void st16(void* p, bf16x8 v) { *(bf16x8*)p = v; }
typedef __amdgpu_buffer_rsrc_t rsrc_t;
DI rsrc_t mk_rsrc(const void* p, unsigned bytes) { return __builtin_amdgcn_make_buffer_rsrc((void*)p, 0, bytes, 0x00020000); }
DI bf16x8 bld16(rsrc_t r, unsigned voff, unsigned soff) {
  return __builtin_bit_cast(bf16x8, __builtin_amdgcn_raw_buffer_load_b128(r, voff, soff, 0));
}

constexpr unsigned L1_E0 = 131072, L1_E1 = L1_E0 + 16384;
constexpr unsigned L1_SJ = L1_E1;
constexpr unsigned L1_PART = L1_E1 + 9216;
constexpr unsigned L1_CS = L1_PART + 2048;
constexpr unsigned K1_LDS = 163840;

__global__ __launch_bounds__(512, 2) void k_proj(const __bf16* __restrict__ x, const __bf16* __restrict__ wqkv,
                                                 const __bf16* __restrict__ wg, const __bf16* __restrict__ wu,
                                                 const __bf16* __restrict__ wd, const __bf16* __restrict__ wo,
                                                 char* __restrict__ ws) {
  const int tid = threadIdx.x, lane = tid & 63;
  const int w = __builtin_amdgcn_readfirstlane(tid >> 6);
  const int mt = w & 3, ng = w >> 2;
  const int bg = blockIdx.x & 7, bi = blockIdx.x >> 3;
  const int rb = (bg & 1) * 8 + (bi & 7), cb = (bg >> 1) * 4 + (bi >> 3);
  const int pp = cb >> 1, odd = cb & 1;
  const int nGU = 3 + odd;
  const int h = lane >> 5;

  const int r8 = lane >> 3, c16 = lane & 7;
  const int c32 = (c16 >> 1) ^ ((r8 >> 1) & 1);
  const char* pGU = (const char*)((c32 < 2 ? wg : wu) + (c32 & 1) * 16 + (c16 & 1) * 8 + (w * 8 + r8) * FF);
  const char* pQ = (const char*)(wqkv + c32 * 16 + (c16 & 1) * 8 + (w * 8 + r8) * (3 * DM));
  const int xrow = w * 8 + r8;
  const char* pX = (const char*)(x + (size_t)(rb * 128 + xrow) * DM + (c16 ^ ((xrow >> 1) & 7)) * 8);
  auto rp_idx = [&](int rr) { return rr < nGU ? 7 * pp + 3 * odd + rr : 3 * pp + (odd ? 2 : rr - 3); };
  auto loadW = [&](int rr, int q, unsigned slot) {
    const int idx = rp_idx(rr);
    const bool gu = rr < nGU;
    const char* p = gu ? pGU : pQ;
    const unsigned stride = gu ? FF * 2 : 3 * DM * 2;
    const unsigned off = (gu ? idx * 64 : idx * 128) + q * 128 * stride;
    glds16(p + off, slot + w * 1024);
    glds16(p + off + 64 * stride, slot + w * 1024 + 8192);
  };
  auto loadX = [&](int j, unsigned slot) {
    glds16(pX + j * 128, slot + w * 1024);
    glds16(pX + j * 128 + 64 * DM * 2, slot + w * 1024 + 8192);
  };
  constexpr unsigned SL = 16384;
  loadW(0, 0, 0 * SL); loadX(0, L1_E0); loadX(1, L1_E1);
  loadW(0, 1, 1 * SL); loadX(2, 2 * SL); loadX(3, 3 * SL);
  loadW(0, 2, 4 * SL); loadX(4, 5 * SL); loadX(5, 6 * SL);
  loadW(0, 3, 7 * SL);

  const int q4 = (lane & 15) >> 2, p4 = lane & 3, blk = (lane >> 4) & 1;
  unsigned wa[4];
  {
    const unsigned b = (8 * h + q4) * 128 + (((2 * blk + ng) ^ (q4 >> 1)) * 32) + p4 * 8;
#pragma unroll
    for (int i = 0; i < 4; ++i) wa[i] = b + ((i ^ mt) * 2048);
  }
  unsigned xa[4];
  {
    const int row = mt * 32 + (lane & 31);
    const unsigned b = row * 128 + ((h ^ ((row >> 1) & 7)) * 16);
#pragma unroll
    for (int i = 0; i < 4; ++i) xa[i] = b ^ ((i ^ mt) * 32);
  }
  auto readW = [&](unsigned slot, int s) {
    const unsigned a = wa[s & 3] + slot + (s >> 2) * 8192;
    return cat8(lds_tr(a), lds_tr(a + 512));
  };
  const bf16x2 ones = {(__bf16)1.0f, (__bf16)1.0f};

  bf16x8 xf[32];
  bf16x8 wf[4];
  f32x16 acc = zero16(), accP;
  float cs = 0.f, s1 = 0.f, s2 = 0.f, s2b = 0.f;
  auto csum_add = [&](bf16x8 f) {
#pragma unroll
    for (int i = 0; i < 4; ++i) cs = dot2(pair(f, i), ones, cs);
  };
  auto put_part = [&]() {
    lds_st<float>(L1_PART + ng * 1024 + (lane & 31) * 32 + (mt * 2 + h) * 4, cs);
    cs = 0.f;
  };

  constexpr unsigned wslot0[4] = {0 * SL, 1 * SL, 4 * SL, 7 * SL};
  constexpr unsigned xslot0[8] = {L1_E0, L1_E1, 2 * SL, 3 * SL, 5 * SL, 6 * SL, 0 * SL, L1_E0};
#pragma unroll
  for (int st = 0; st < 4; ++st) {
    if (st == 0) wait_vm_barrier<14>();
    if (st == 1) {
      wait_vm_barrier<8>();
      loadX(6, 0 * SL); loadX(7, L1_E0);
#pragma unroll
      for (int e = 0; e < 2; ++e) {
        if (e == 0 || w == 0) {
          const int f = blockIdx.x * 9 + (e ? 8 : w);
          const bool isd = f < 1792;
          const int f2 = isd ? f : f - 1792;
          const int nt = isd ? f2 / 112 : f2 >> 5;
          const int ks = isd ? f2 - nt * 112 : f2 & 31;
          const __bf16* src = (isd ? wd : wo) + (size_t)(ks * 16 + (lane >> 2)) * DM + nt * 32 + (lane & 3) * 8;
          glds16(src, L1_SJ + (e ? 8 : w) * 1024);
        }
      }
    }
    if (st == 2) {
      wait_vm_barrier<7>();
      loadW(1, 0, 1 * SL); loadW(1, 1, 2 * SL); loadW(1, 2, 3 * SL);
    }
    if (st == 3) {
      wait_vm_barrier<6>();
      loadW(1, 3, 4 * SL); loadW(2, 0, 5 * SL); loadW(2, 1, 6 * SL);
#pragma unroll
      for (int e = 0; e < 2; ++e) {
        if (e == 0 || w == 0) {
          const int f = blockIdx.x * 9 + (e ? 8 : w);
          const unsigned a = L1_SJ + (e ? 8 : w) * 1024 + (4 * h + q4) * 64 + blk * 32 + p4 * 8;
          st16(ws + WS_WD + (size_t)f * 1024 + lane * 16, cat8(lds_tr(a), lds_tr(a + 512)));
        }
      }
    }
#pragma unroll
    for (int s = 0; s < 8; ++s) {
      const int gs = st * 8 + s;
      xf[gs] = lds_ld<bf16x8>(xa[s & 3] + xslot0[st * 2 + (s >> 2)]);
      wf[gs & 3] = readW(wslot0[st], s);
      if (gs >= 3) acc = mfma(wf[(gs - 3) & 3], xf[gs - 3], acc);
      if ((gs & 3) == 0) csum_add(wf[0]);
#pragma unroll
      for (int i = 0; i < 4; ++i) {
        s1 = dot2(pair(xf[gs], i), ones, s1);
        if (i & 1) s2b = dot2(pair(xf[gs], i), pair(xf[gs], i), s2b);
        else s2 = dot2(pair(xf[gs], i), pair(xf[gs], i), s2);
      }
    }
  }
  put_part();
  float mean, rstd;
  {
    const float m = xhalf_sum(s1) * (1.0f / DM);
    const float var = fmaxf(xhalf_sum(s2 + s2b) * (1.0f / DM) - m * m, 0.f);
    mean = bfround(m);
    const float den = bfround(__builtin_sqrtf(bfround(bfround(var) + 1e-5f)));
    rstd = 1.0f / den;
  }
  const int tt = rb * 4 + mt;

#pragma nounroll
  for (int r = 1; r <= 5; ++r) {
    if (r < 4) wait_vm_barrier<8>(); else wait_vm_barrier<0>();
    const unsigned sl0 = ((4 * r + 5) & 7) * SL;
    if (r <= 3) { loadW(r + 1, 2, ((4 * r + 11) & 7) * SL); loadW(r + 1, 3, ((4 * r + 12) & 7) * SL); }
    wf[0] = readW(sl0, 0); accP = mfma(wf[1], xf[29], acc);
    wf[1] = readW(sl0, 1); accP = mfma(wf[2], xf[30], accP);
    wf[2] = readW(sl0, 2); accP = mfma(wf[3], xf[31], accP);
#pragma nounroll
    for (int pass = 0; pass < 2; ++pass) {
      if (pass == ng || r == 5) {
        const int rr = r - 1;
        const int idx = rp_idx(rr);
        float c;
        {
          const unsigned a = L1_PART + ng * 1024 + (lane & 31) * 32;
          const f32x4 u = lds_ld<f32x4>(a), v = lds_ld<f32x4>(a + 16);
          c = ((u[0] + u[1]) + (u[2] + u[3])) + ((v[0] + v[1]) + (v[2] + v[3]));
        }
        lds_st<float>(L1_CS + w * 128 + (lane & 31) * 4, c);
        f32x16 y;
#pragma unroll
        for (int g = 0; g < 4; ++g) {
          const f32x4 cv = lds_ld<f32x4>(L1_CS + w * 128 + (8 * g + 4 * h) * 4);
#pragma unroll
          for (int i = 0; i < 4; ++i) y[4 * g + i] = __builtin_fmaf(-mean, cv[i], accP[4 * g + i]);
        }
        if (rr < nGU) {
          const float rg = rstd * -1.4426950408889634f;
          bf16x8 o;
#pragma unroll
          for (int j = 0; j < 8; ++j) {
            const float e = __builtin_amdgcn_exp2f(y[j] * rg);
            const float sg = __builtin_amdgcn_rcpf(1.0f + e);
            o[j] = (__bf16)(y[8 + j] * rstd * sg);
          }
          st16(ws + WS_H + (size_t)(tt * 112 + 2 * idx + ng) * 1024 + lane * 16, o);
        } else {
          const int which = idx >> 3;
          const int ha = (idx & 7) * 4 + ng;
          const float sc = which == 0 ? rstd * (0.25f * 1.4426950408889634f) : rstd;
          bf16x8 oa, ob;
#pragma unroll
          for (int j = 0; j < 8; ++j) { oa[j] = (__bf16)(y[j] * sc); ob[j] = (__bf16)(y[8 + j] * sc); }
          if (which == 0) {
            char* dst = ws + WS_Q + (size_t)(ha * 64 + tt) * 1024 + lane * 16;
            st16(dst, oa); st16(dst + 2 * 64 * 1024, ob);
          } else if (which == 1) {
            char* dst = ws + WS_KV + (size_t)(ha * 64 + tt) * 2048 + lane * 16;
            st16(dst, oa); st16(dst + 2 * 64 * 2048, ob);
          } else {
            const unsigned sb = L1_E0 + w * 2048;
            const unsigned wadr = sb + (lane & 31) * 64 + h * 8;
            lds_st<bf16x4>(wadr, __builtin_shufflevector(oa, oa, 0, 1, 2, 3));
            lds_st<bf16x4>(wadr + 16, __builtin_shufflevector(oa, oa, 4, 5, 6, 7));
            lds_st<bf16x4>(wadr + 32, __builtin_shufflevector(ob, ob, 0, 1, 2, 3));
            lds_st<bf16x4>(wadr + 48, __builtin_shufflevector(ob, ob, 4, 5, 6, 7));
            const unsigned ra = sb + (8 * h + q4) * 64 + blk * 32 + p4 * 8;
            char* dst = ws + WS_KV + (size_t)((ha + 2 * blk) * 64 + tt) * 2048 + 1024 + h * 256 + (lane & 15) * 16;
            st16(dst, cat8(lds_tr(ra), lds_tr(ra + 256)));
            st16(dst + 512, cat8(lds_tr(ra + 1024), lds_tr(ra + 1280)));
          }
        }
        if (r == 5) return;
      }
      if (pass == 0) {
        csum_add(wf[0]);
        const unsigned sl1 = ((4 * r + 6) & 7) * SL;
#pragma unroll
        for (int s = 3; s < 16; ++s) {
          wf[s & 3] = readW(s < 8 ? sl0 : sl1, s & 7);
          acc = mfma(wf[(s - 3) & 3], xf[s - 3], s == 3 ? zero16() : acc);
          if ((s & 3) == 0) csum_add(wf[0]);
        }
      }
    }
    if (r < 4) wait_vm_barrier<8>();
    if (r <= 2) { loadW(r + 2, 0, ((4 * r + 13) & 7) * SL); loadW(r + 2, 1, ((4 * r + 14) & 7) * SL); }
    {
      const unsigned sl2 = ((4 * r + 7) & 7) * SL, sl3 = ((4 * r + 8) & 7) * SL;
#pragma unroll
      for (int s = 16; s < 32; ++s) {
        wf[s & 3] = readW(s < 24 ? sl2 : sl3, s & 7);
        acc = mfma(wf[(s - 3) & 3], xf[s - 3], acc);
        if ((s & 3) == 0) csum_add(wf[0]);
      }
    }
    if (r == 4) asm volatile("s_barrier" ::: "memory");
    put_part();
  }
}

constexpr unsigned TB = 2080;
constexpr unsigned L2_Q = 66 * TB;
constexpr unsigned L2_X = L2_Q + 8192;
constexpr unsigned L2_F = L2_X + 16384;
constexpr unsigned K2_LDS = L2_F + 256;

DI f32x16 pv(bf16x8 v, f32x8 p, f32x16 o) {
  return __builtin_amdgcn_smfmac_f32_32x32x32_bf16(v, __builtin_bit_cast(bf16x16, p), o, 0xDDDD, 0, 0);
}
DI f32x8 exp8(f32x8 s) { f32x8 r; for (int i = 0; i < 8; ++i) r[i] = __builtin_amdgcn_exp2f(s[i]); return r; }
DI f32x8 lo8(f32x16 s) { return __builtin_shufflevector(s, s, 0, 1, 2, 3, 4, 5, 6, 7); }
DI f32x8 hi8(f32x16 s) { return __builtin_shufflevector(s, s, 8, 9, 10, 11, 12, 13, 14, 15); }
DI float max16(f32x16 s) {
  float m = fmaxf(s[0], s[1]);
  for (int i = 2; i < 16; i += 2) m = fmaxf(m, fmaxf(s[i], s[i + 1]));
  return m;
}
DI void lds_signal(unsigned a, int lane) {
  if (lane == 0) __hip_atomic_fetch_add((AS3 unsigned*)a, 1u, __ATOMIC_RELAXED, __HIP_MEMORY_SCOPE_WORKGROUP);
}
DI void lds_wait_ge(unsigned a, unsigned n) {
  while (__builtin_amdgcn_readfirstlane(*(volatile AS3 unsigned*)a) < n) __builtin_amdgcn_s_sleep(1);
  asm volatile("" ::: "memory");
}
#define SGB(m, n) __builtin_amdgcn_sched_group_barrier(m, n, 0)

__global__ __launch_bounds__(768, 3) void k_attn(char* __restrict__ ws) {
  const int tid = threadIdx.x, lane = tid & 63;
  const int w = __builtin_amdgcn_readfirstlane(tid >> 6);
  const int bg = blockIdx.x & 7, bi = blockIdx.x >> 3;
  const int h = lane >> 5;
  if (w < 8) {
    const int head = bg * 4 + (bi >> 3), qt = (bi & 7) * 8 + w;
    glds16(ws + WS_Q + (size_t)(head * 64 + qt) * 1024 + lane * 16, L2_Q + w * 1024);
    const char* kv = ws + WS_KV + (size_t)head * 131072 + w * 1024 + lane * 16;
    const unsigned dst = (w >> 1) * TB + (w & 1) * 1024;
#pragma unroll
    for (int i = 0; i < 16; ++i) glds16(kv + i * 8192, dst + i * 4 * TB);
    if (tid < 132) {
      const unsigned v = (tid & 1) ? 0u : 0x3f803f80u;
      u32x4 c = {v, v, v, v};
      lds_st<u32x4>((tid >> 1) * TB + 2048 + (tid & 1) * 16, c);
    }
    if (tid >= 256 && tid < 320) lds_st<unsigned>(L2_F + (tid - 256) * 4, 0u);
    asm volatile("s_waitcnt lgkmcnt(0)\n\ts_barrier" ::: "memory");
    wait_vm<13>();
    lds_signal(L2_F, lane);
    const bf16x8 q = lds_ld<bf16x8>(L2_Q + w * 1024 + lane * 16);
    const int d = lane & 31;
    const unsigned ka = lane * 16;
    const unsigned cslot = (d == 16 || d == 20) ? 2048 : 2064;
    const unsigned va0 = d < 16 ? 1024 + h * 256 + d * 16 : cslot;
    const unsigned va1 = d < 16 ? 1536 + h * 256 + d * 16 : cslot;
#define LK(t) lds_ld<bf16x8>(ka + (t) * TB)
#define LV0(t) lds_ld<bf16x8>(va0 + (t) * TB)
#define LV1(t) lds_ld<bf16x8>(va1 + (t) * TB)
    lds_wait_ge(L2_F, 8);
    float m = xhalf_max(fmaxf(max16(mfma(LK(0), q, zero16())), max16(mfma(LK(1), q, zero16()))));
    f32x16 o;
    bool full = false;
#pragma nounroll
    for (int attempt = 0; attempt < 2; ++attempt) {
      f32x16 negm;
#pragma unroll
      for (int i = 0; i < 16; ++i) negm[i] = -m;
      o = zero16();
      f32x16 s = mfma(LK(0), q, negm);
      bf16x8 kn = LK(1);
      bf16x8 v0 = LV0(0), v1 = LV1(0);
      f32x8 ph;
#pragma unroll
      for (int i = 0; i < 8; ++i) ph[i] = 0.f;
      bf16x8 v1p = v1;
#pragma nounroll
      for (int t0 = 0; t0 < 64; t0 += 4) {
        if (t0 == 8 && !full) {
          wait_vm<0>();
          lds_signal(L2_F + 4, lane);
          lds_wait_ge(L2_F + 4, 8);
          full = true;
        }
#pragma unroll
        for (int u = 0; u < 4; ++u) {
          const int t = t0 + u;
          f32x16 sn = mfma(kn, q, negm);
          bf16x8 kn2 = LK(t + 2);
          bf16x8 v0n = LV0(t + 1), v1n = LV1(t + 1);
          f32x8 pl = exp8(lo8(s));
          o = pv(v1p, ph, o);
          ph = exp8(hi8(s));
          o = pv(v0, pl, o);
          v1p = v1; v0 = v0n; v1 = v1n; kn = kn2; s = sn;
          SGB(0x8, 1); SGB(0x100, 1); SGB(0x400, 5); SGB(0x8, 1); SGB(0x100, 1); SGB(0x400, 5); SGB(0x8, 1); SGB(0x100, 1); SGB(0x400, 6);
        }
      }
      o = pv(v1p, ph, o);
      float mx = o[8];
#pragma unroll
      for (int i = 0; i < 8; ++i) mx = fmaxf(mx, __builtin_fabsf(o[i]) * 0x1p-60f);
      bool ok = true;
#pragma unroll
      for (int i = 0; i <= 8; ++i) ok = ok && (o[i] == o[i]);
      ok = ok && (mx <= 0x1p60f);
      if (attempt == 1 || __all(ok)) break;
      float tm = m;
#pragma nounroll
      for (int t = 0; t < 64; ++t) tm = fmaxf(tm, max16(mfma(LK(t), q, zero16())));
      m = xhalf_max(tm);
    }
    const float inv = 1.0f / o[8];
    bf16x8 ob;
#pragma unroll
    for (int j = 0; j < 8; ++j) ob[j] = (__bf16)(o[j] * inv);
    st16(ws + WS_O + (size_t)(qt * 32 + head) * 1024 + lane * 16, ob);
  } else {
    const int u = w - 8;
    const int rt = bg * 4 + (bi & 3), ct = bi >> 2;
    const rsrc_t rh = mk_rsrc(ws + WS_H, 64 * 112 * 1024), rw = mk_rsrc(ws + WS_WD, 1792 * 1024);
    const unsigned h0 = ((rt * 2 + (u & 1)) * 112 + u * 28) * 1024, h1 = ((rt * 2 + ((u & 1) ^ 1)) * 112 + u * 28) * 1024;
    const unsigned w0 = ((ct * 2 + (u >> 1)) * 112 + u * 28) * 1024, w1 = ((ct * 2 + ((u >> 1) ^ 1)) * 112 + u * 28) * 1024;
    const unsigned vo = lane * 16;
    constexpr int PD = 5;
    bf16x8 fh[PD][2], fw[PD][2];
#pragma unroll
    for (int s = 0; s < PD; ++s) {
      fh[s][0] = bld16(rh, vo, h0 + s * 1024); fh[s][1] = bld16(rh, vo, h1 + s * 1024);
      fw[s][0] = bld16(rw, vo, w0 + s * 1024); fw[s][1] = bld16(rw, vo, w1 + s * 1024);
    }
    asm volatile("s_barrier" ::: "memory");
    f32x16 acc[4];
#pragma unroll
    for (int i = 0; i < 4; ++i) acc[i] = zero16();
#pragma unroll
    for (int s = 0; s < 28; ++s) {
      const int b = s % PD;
      acc[0] = mfma(fw[b][0], fh[b][0], acc[0]);
      acc[1] = mfma(fw[b][0], fh[b][1], acc[1]);
      acc[2] = mfma(fw[b][1], fh[b][0], acc[2]);
      acc[3] = mfma(fw[b][1], fh[b][1], acc[3]);
      if (s + PD < 28) {
        fh[b][0] = bld16(rh, vo, h0 + (s + PD) * 1024); fh[b][1] = bld16(rh, vo, h1 + (s + PD) * 1024);
        fw[b][0] = bld16(rw, vo, w0 + (s + PD) * 1024); fw[b][1] = bld16(rw, vo, w1 + (s + PD) * 1024);
      }
      __builtin_amdgcn_sched_barrier(0);
    }
    f32x16 r0 = acc[0];
    const unsigned mys = L2_X + u * 4096 + lane * 16;
#pragma unroll
    for (int r = 1; r < 4; ++r) {
      if (r > 1) lds_wait_ge(L2_F + 64 + u * 4, r - 1);
#pragma unroll
      for (int g = 0; g < 4; ++g) {
        f32x4 t = {acc[r][4 * g], acc[r][4 * g + 1], acc[r][4 * g + 2], acc[r][4 * g + 3]};
        lds_st<f32x4>(mys + g * 1024, t);
      }
      asm volatile("s_waitcnt lgkmcnt(0)" ::: "memory");
      lds_signal(L2_F + 32 + u * 4, lane);
      const int src = u ^ r;
      lds_wait_ge(L2_F + 32 + src * 4, r);
#pragma unroll
      for (int g = 0; g < 4; ++g) {
        const f32x4 t = lds_ld<f32x4>(L2_X + src * 4096 + lane * 16 + g * 1024);
#pragma unroll
        for (int i = 0; i < 4; ++i) r0[4 * g + i] += t[i];
      }
      asm volatile("s_waitcnt lgkmcnt(0)" ::: "memory");
      lds_signal(L2_F + 64 + src * 4, lane);
    }
    __bf16* gp = (__bf16*)(ws + WS_G) + (size_t)((rt * 2 + (u & 1)) * 32 + (lane & 31)) * DM + (ct * 2 + (u >> 1)) * 32 + 4 * h;
#pragma unroll
    for (int g = 0; g < 4; ++g) {
      bf16x4 ov;
#pragma unroll
      for (int i = 0; i < 4; ++i) {
        const float z = r0[4 * g + i];
        ov[i] = (__bf16)(z * __builtin_amdgcn_rcpf(1.0f + __builtin_amdgcn_exp2f(z * -1.4426950408889634f)));
      }
      *(bf16x4*)(gp + 8 * g) = ov;
    }
  }
}

constexpr unsigned K3_ROW = 272;
constexpr unsigned K3_LDS = 4 * 64 * K3_ROW;

__global__ __launch_bounds__(256, 1) void k_out(const __bf16* __restrict__ x, const char* __restrict__ ws,
                                                __bf16* __restrict__ out) {
  const int tid = threadIdx.x, lane = tid & 63;
  const int u = __builtin_amdgcn_readfirstlane(tid >> 6);
  const int bg = blockIdx.x & 7, bi = blockIdx.x >> 3;
  const int h = lane >> 5;
  const int rt = bg * 4 + (bi & 3), ct = bi >> 2;
  const rsrc_t ro = mk_rsrc(ws + WS_O, 2097152), rw = mk_rsrc(ws + WS_WO, 512 * 1024);
  const unsigned o0s = (rt * 2 * 32 + u * 8) * 1024, w0s = (ct * 2 * 32 + u * 8) * 1024;
  const unsigned vo = lane * 16;
  bf16x8 fo[8][2], fw[8][2];
#pragma unroll
  for (int s = 0; s < 8; ++s) {
    fo[s][0] = bld16(ro, vo, o0s + s * 1024); fo[s][1] = bld16(ro, vo, o0s + (32 + s) * 1024);
    fw[s][0] = bld16(rw, vo, w0s + s * 1024); fw[s][1] = bld16(rw, vo, w0s + (32 + s) * 1024);
  }
  const int row = tid >> 2, cg = (tid & 3) * 16;
  const size_t eo = (size_t)(rt * 64 + row) * DM + ct * 64 + cg;
  const bf16x8 x0 = *(const bf16x8*)(x + eo), x1 = *(const bf16x8*)(x + eo + 8);
  const __bf16* gp = (const __bf16*)(ws + WS_G);
  const bf16x8 g0 = *(const bf16x8*)(gp + eo), g1 = *(const bf16x8*)(gp + eo + 8);
  __builtin_amdgcn_sched_barrier(0);
  f32x16 acc[4];
#pragma unroll
  for (int i = 0; i < 4; ++i) acc[i] = zero16();
#pragma unroll
  for (int s = 0; s < 8; ++s) {
    acc[0] = mfma(fw[s][0], fo[s][0], acc[0]);
    acc[1] = mfma(fw[s][0], fo[s][1], acc[1]);
    acc[2] = mfma(fw[s][1], fo[s][0], acc[2]);
    acc[3] = mfma(fw[s][1], fo[s][1], acc[3]);
  }
#pragma unroll
  for (int i = 0; i < 4; ++i) {
    const unsigned a = u * 64 * K3_ROW + ((i & 1) * 32 + (lane & 31)) * K3_ROW + ((i >> 1) * 32 + 4 * h) * 4;
#pragma unroll
    for (int g = 0; g < 4; ++g) {
      f32x4 t = {acc[i][4 * g], acc[i][4 * g + 1], acc[i][4 * g + 2], acc[i][4 * g + 3]};
      lds_st<f32x4>(a + g * 32, t);
    }
  }
  __syncthreads();
  float sres[16];
#pragma unroll
  for (int g = 0; g < 4; ++g) {
    const unsigned a = row * K3_ROW + cg * 4 + g * 16;
    const f32x4 t0 = lds_ld<f32x4>(a), t1 = lds_ld<f32x4>(a + 64 * K3_ROW), t2 = lds_ld<f32x4>(a + 128 * K3_ROW),
                t3 = lds_ld<f32x4>(a + 192 * K3_ROW);
#pragma unroll
    for (int i = 0; i < 4; ++i) sres[4 * g + i] = (t0[i] + t1[i]) + (t2[i] + t3[i]);
  }
  bf16x8 o0, o1;
#pragma unroll
  for (int j = 0; j < 8; ++j) {
    o0[j] = (__bf16)__builtin_fmaf(sres[j], (float)g0[j], (float)x0[j]);
    o1[j] = (__bf16)__builtin_fmaf(sres[8 + j], (float)g1[j], (float)x1[j]);
  }
  *(bf16x8*)(out + eo) = o0;
  *(bf16x8*)(out + eo + 8) = o1;
}

}

extern "C" void kernel_launch(void* const* d_in, const int* in_sizes, int n_in, void* d_out, int out_size,
                              void* d_ws, size_t ws_size, hipStream_t stream) {
  const __bf16* x = (const __bf16*)d_in[0];
  const __bf16* wqkv = (const __bf16*)d_in[1];
  const __bf16* wo = (const __bf16*)d_in[2];
  const __bf16* wg = (const __bf16*)d_in[4];
  const __bf16* wu = (const __bf16*)d_in[5];
  const __bf16* wd = (const __bf16*)d_in[6];
  char* ws = (char*)d_ws;
  (void)hipFuncSetAttribute((const void*)k_proj, hipFuncAttributeMaxDynamicSharedMemorySize, K1_LDS);
  (void)hipFuncSetAttribute((const void*)k_attn, hipFuncAttributeMaxDynamicSharedMemorySize, K2_LDS);
  (void)hipFuncSetAttribute((const void*)k_out, hipFuncAttributeMaxDynamicSharedMemorySize, K3_LDS);
  k_proj<<<256, 512, K1_LDS, stream>>>(x, wqkv, wg, wu, wd, wo, ws);
  k_attn<<<256, 768, K2_LDS, stream>>>(ws);
  k_out<<<256, 256, K3_LDS, stream>>>(x, ws, (__bf16*)d_out);
}
```

```cpp
#include <hip/hip_runtime.h>
#include <hip/hip_bf16.h>

typedef __attribute__((ext_vector_type(2))) __bf16 bf16x2;
typedef __attribute__((ext_vector_type(4))) __bf16 bf16x4;
typedef __attribute__((ext_vector_type(8))) __bf16 bf16x8;
typedef __attribute__((ext_vector_type(16))) __bf16 bf16x16;
typedef __attribute__((ext_vector_type(4))) float f32x4;
typedef __attribute__((ext_vector_type(8))) float f32x8;
typedef __attribute__((ext_vector_type(16))) float f32x16;
typedef __attribute__((ext_vector_type(4))) short s16x4;
typedef __attribute__((ext_vector_type(2))) unsigned u32x2;
typedef __attribute__((ext_vector_type(4))) unsigned u32x4;
#define AS3 __attribute__((address_space(3)))
#define DI __device__ __forceinline__

#define STAMP_DECL
#define STAMP(kern, i)
#define STAMP_FLUSH(kern)

namespace {

constexpr int SEQ = 2048, DM = 512, FF = 1792;
constexpr size_t WS_Q = 0;
constexpr size_t WS_KV = WS_Q + 2097152;
constexpr size_t WS_H = WS_KV + 4194304;
constexpr size_t WS_WD = WS_H + 64 * 112 * 1024;
constexpr size_t WS_WO = WS_WD + 1792 * 1024;
constexpr size_t WS_O = WS_WO + 512 * 1024;
constexpr size_t WS_F = WS_O + 2097152;
constexpr size_t WS_F1 = WS_F + 1024, WS_F2 = WS_F + 4096, WS_F3 = WS_F + 8192;
constexpr size_t WS_END = WS_F + 8192 + 16384;

template <class T> DI T lds_ld(unsigned a) { return *(const AS3 T*)(a); }
template <class T> DI void lds_st(unsigned a, T v) { *(AS3 T*)(a) = v; }
DI bf16x4 lds_tr(unsigned a) { return __builtin_amdgcn_ds_read_tr16_b64_v4bf16((AS3 bf16x4*)(a)); }
DI bf16x8 cat8(bf16x4 a, bf16x4 b) { return __builtin_shufflevector(a, b, 0, 1, 2, 3, 4, 5, 6, 7); }
DI f32x16 mfma(bf16x8 a, bf16x8 b, f32x16 c) { return __builtin_amdgcn_mfma_f32_32x32x16_bf16(a, b, c, 0, 0, 0); }
DI f32x16 zero16() { f32x16 z; for (int i = 0; i < 16; ++i) z[i] = 0.f; return z; }
DI void glds16(const void* gsrc, unsigned lds_dst) {
#if defined(__HIP_DEVICE_COMPILE__)
  asm volatile("s_mov_b32 m0, %1\n\ts_nop 0\n\tglobal_load_lds_dwordx4 %0, off" ::"v"(gsrc), "s"(lds_dst) : "memory");
#endif
}
template <int N> DI void wait_vm_barrier() {
  asm volatile("s_waitcnt vmcnt(%0) lgkmcnt(0)\n\ts_barrier" ::"i"(N) : "memory");
}
template <int N> DI void wait_vm() { asm volatile("s_waitcnt vmcnt(%0)" ::"i"(N) : "memory"); }
DI float dot2(bf16x2 a, bf16x2 b, float c) { return __builtin_amdgcn_fdot2_f32_bf16(a, b, c, false); }
DI bf16x2 pair(bf16x8 v, int i) { bf16x2 r = {v[2 * i], v[2 * i + 1]}; return r; }
DI void pin(float& a) {
#if defined(__HIP_DEVICE_COMPILE__)
  asm volatile("" : "+v"(a));
#endif
}
DI void pin2(bf16x2& a) {
#if defined(__HIP_DEVICE_COMPILE__)
  asm volatile("" : "+v"(a));
#endif
}
DI void pin4(f32x4& a) {
#if defined(__HIP_DEVICE_COMPILE__)
  asm volatile("" : "+v"(a));
#endif
}
DI void pin16(f32x16& a) {
#if defined(__HIP_DEVICE_COMPILE__)
  asm volatile("" : "+v"(a));
#endif
}
template <int V> struct IC { static constexpr int value = V; };
template <int B, int E, class F> DI void sfor(F&& f) {
  if constexpr (B < E) { f(IC<B>{}); sfor<B + 1, E>(f); }
}
#define AINL __attribute__((always_inline))
#define SB0() __builtin_amdgcn_sched_barrier(0)
DI unsigned vxor(unsigned a, unsigned c) {
  unsigned r = 0;
#if defined(__HIP_DEVICE_COMPILE__)
  asm volatile("v_xor_b32 %0, %1, %2" : "=v"(r) : "i"(c), "v"(a));
#endif
  return r;
}
DI float bfround(float x) { return (float)(__bf16)x; }
DI float xhalf_sum(float v) {
  auto r = __builtin_amdgcn_permlane32_swap(__float_as_uint(v), __float_as_uint(v), false, false);
  return __uint_as_float(r[0]) + __uint_as_float(r[1]);
}
DI float xhalf_max(float v) {
  auto r = __builtin_amdgcn_permlane32_swap(__float_as_uint(v), __float_as_uint(v), false, false);
  return fmaxf(__uint_as_float(r[0]), __uint_as_float(r[1]));
}
DI void st16(void* p, bf16x8 v) { *(bf16x8*)p = v; }
typedef __amdgpu_buffer_rsrc_t rsrc_t;
DI rsrc_t mk_rsrc(const void* p, unsigned bytes) { return __builtin_amdgcn_make_buffer_rsrc((void*)p, 0, bytes, 0x00020000); }
#define ST_AUX 16
DI void bst16(rsrc_t r, unsigned voff, unsigned soff, bf16x8 v) {
  __builtin_amdgcn_raw_buffer_store_b128(__builtin_bit_cast(u32x4, v), r, voff, soff, ST_AUX);
}
DI bf16x8 bld16(rsrc_t r, unsigned voff, unsigned soff) {
  return __builtin_bit_cast(bf16x8, __builtin_amdgcn_raw_buffer_load_b128(r, voff, soff, 16));
}

constexpr unsigned SL = 16384;
constexpr unsigned L1_E0 = 8 * SL, L1_E1 = 9 * SL;
constexpr unsigned L1_SJ = L1_E1;
constexpr unsigned L1_PART = L1_E1 + 9216;
constexpr unsigned LDS_BYTES = 163840;
constexpr unsigned blk_slot(int b) { return (b & 7) * SL; }
constexpr int k1_wait(int st) { return st <= 7 ? 4 : st == 8 ? 6 : st == 9 ? 8 : st <= 14 ? 10 : 0; }
constexpr unsigned TB = 2080;
constexpr unsigned L2_Q = 66 * TB;
constexpr unsigned RROW = 272, RPART = 64 * RROW;
constexpr unsigned SPIN_CAP = 1u << 18;

template <int V, int L> DI void wait_barrier() {
  asm volatile("s_waitcnt vmcnt(%0) lgkmcnt(%1)\n\ts_barrier" ::"i"(V), "i"(L) : "memory");
}
template <bool SC1> DI void glds16s(const void* sbase, unsigned voff, unsigned lds_dst) {
#if defined(__HIP_DEVICE_COMPILE__)
  if (SC1) asm volatile("s_mov_b32 m0, %2\n\ts_nop 0\n\tglobal_load_lds_dwordx4 %0, %1 sc1" ::"v"(voff), "s"(sbase), "s"(lds_dst) : "memory");
  else asm volatile("s_mov_b32 m0, %2\n\ts_nop 0\n\tglobal_load_lds_dwordx4 %0, %1" ::"v"(voff), "s"(sbase), "s"(lds_dst) : "memory");
#endif
}
DI void pinu(unsigned& a) {
#if defined(__HIP_DEVICE_COMPILE__)
  asm volatile("" : "+v"(a));
#endif
}
typedef unsigned long long u64;
typedef __attribute__((address_space(1))) u64 gu64;
DI u64 flag_ld(const char* p) { return __hip_atomic_load((gu64*)p, __ATOMIC_RELAXED, __HIP_MEMORY_SCOPE_AGENT); }
DI void flag_st(char* p, u64 v) { __hip_atomic_store((gu64*)p, v, __ATOMIC_RELAXED, __HIP_MEMORY_SCOPE_AGENT); }
DI void flag_wait(const char* p, u64 epoch) {
  for (unsigned spins = 0; spins < SPIN_CAP; ++spins) {
    if (__all(flag_ld(p) == epoch)) break;
    __builtin_amdgcn_s_sleep(2);
  }
  asm volatile("" ::: "memory");
}
DI f32x16 pv(bf16x8 v, f32x8 p, f32x16 o) {
  return __builtin_amdgcn_smfmac_f32_32x32x32_bf16(v, __builtin_bit_cast(bf16x16, p), o, 0xDDDD, 0, 0);
}
DI f32x8 exp8(f32x8 s) { f32x8 r; for (int i = 0; i < 8; ++i) r[i] = __builtin_amdgcn_exp2f(s[i]); return r; }
DI f32x8 lo8(f32x16 s) { return __builtin_shufflevector(s, s, 0, 1, 2, 3, 4, 5, 6, 7); }
DI f32x8 hi8(f32x16 s) { return __builtin_shufflevector(s, s, 8, 9, 10, 11, 12, 13, 14, 15); }
DI float max16(f32x16 s) {
  float m = fmaxf(s[0], s[1]);
  for (int i = 2; i < 16; i += 2) m = fmaxf(m, fmaxf(s[i], s[i + 1]));
  return m;
}
#define SGB(m, n) __builtin_amdgcn_sched_group_barrier(m, n, 0)

__global__ __launch_bounds__(512, 2) void k_fused(const __bf16* __restrict__ x, const __bf16* __restrict__ wqkv,
                                                  const __bf16* __restrict__ wg, const __bf16* __restrict__ wu,
                                                  const __bf16* __restrict__ wd, const __bf16* __restrict__ wo,
                                                  char* __restrict__ ws, __bf16* __restrict__ out) {
  const int tid = threadIdx.x, lane = tid & 63;
  const int w = __builtin_amdgcn_readfirstlane(tid >> 6);
  const int bg = blockIdx.x & 7, bi = blockIdx.x >> 3;
  const unsigned l16 = lane * 16;
  const rsrc_t rws = mk_rsrc(ws, (unsigned)WS_F);
  constexpr float LOG2E = 1.4426950408889634f;
  u64 epoch;
  STAMP_DECL; STAMP(0, 0);

  {
    const int mt = w & 3, ng = w >> 2;
    const int perm = mt ^ (2 * ng);
    const int rb = (bg & 1) * 8 + (bi & 7), grp = bg >> 1, cj = bi >> 3;
    const bool typeB = cj == 3;
    const int dblB = 7 * grp + 2 * cj;
    const int dblA = typeB ? 6 * grp + 4 : dblB + 1;
    const int sngl = 6 * grp + cj;
    const int rot = (rb + 2 * cj) & 7;
    const int h = lane >> 5;

    constexpr unsigned SG = FF * 2, SQ = 3 * DM * 2;
    const int r8 = lane >> 3, c8 = lane & 7;
    const unsigned vcol = (((c8 >> 1) ^ ((r8 >> 1) & 1)) * 32) + (c8 & 1) * 16;
    const unsigned vG = r8 * SG + vcol, vQ = r8 * SQ + vcol;
    const unsigned vX = r8 * (DM * 2) + ((c8 ^ ((w * 4 + (r8 >> 1)) & 7)) * 16);
    const char* gBA = (const char*)wg + w * 8 * SG + dblB * 128;
    const char* gBB = (const char*)wu + w * 8 * SG + dblB * 128;
    const char* gAA = typeB ? (const char*)wqkv + w * 8 * SQ + dblA * 128 : gBA + 128;
    const char* gAB = typeB ? gAA + 128 : gBB + 128;
    const char* gS = (const char*)wqkv + w * 8 * SQ + sngl * 128;
    const char* gX = (const char*)x + (size_t)(rb * 128 + w * 8) * (DM * 2);
    const unsigned strA = typeB ? SQ : SG;
    const unsigned vA = typeB ? vQ : vG;
    auto load_blk = [&](int b) AINL {
      const unsigned dst = blk_slot(b) + w * 1024;
      if (b < 16) {
        const unsigned kc = ((b >> 1) + rot) & 7;
        if (b & 1) { glds16s<false>(gX + kc * 128, vX, dst); glds16s<false>(gX + kc * 128 + 64 * DM * 2, vX, dst + 8192); }
        else { glds16s<false>(gAA + kc * 64 * strA, vA, dst); glds16s<false>(gAB + kc * 64 * strA, vA ^ 64, dst + 8192); }
      } else if (b < 20) {
        const unsigned k0 = (2 * (b - 16) + rot) & 7, k1 = (2 * (b - 16) + 1 + rot) & 7;
        glds16s<false>(gS + k0 * (64 * SQ), vQ, dst); glds16s<false>(gS + k1 * (64 * SQ), vQ, dst + 8192);
      } else {
        const unsigned kc = (b - 20 + rot) & 7;
        glds16s<false>(gBA + kc * (64 * SG), vG, dst); glds16s<false>(gBB + kc * (64 * SG), vG ^ 64, dst + 8192);
      }
    };
    load_blk(0); load_blk(1);
    asm volatile("s_barrier" ::: "memory");
    load_blk(2); load_blk(3);
#if defined(__HIP_DEVICE_COMPILE__)
    asm volatile("s_load_dwordx2 %0, %1, 0x0\n\ts_waitcnt lgkmcnt(0)" : "=s"(epoch) : "s"(ws + WS_F) : "memory");
#endif
    epoch += 0x9E3779B97F4A7C15ull;
    const char* sjsrc[2];
#pragma unroll
    for (int e = 0; e < 2; ++e) {
      const int f = blockIdx.x * 9 + (e ? 8 : w);
      const bool isd = f < 1792;
      const int f2 = isd ? f : f - 1792;
      const int nt = isd ? f2 / 112 : f2 >> 5;
      const int ks = isd ? f2 - nt * 112 : f2 & 31;
      sjsrc[e] = (const char*)(isd ? wd : wo) + ks * 16 * (DM * 2) + nt * 64;
    }

    const int q4 = (lane & 15) >> 2, p4 = lane & 3, blk = (lane >> 4) & 1;
    unsigned aD[2][4];
    unsigned aS[4];
    auto init_aD = [&](unsigned ln, unsigned win) AINL {
      const unsigned q_ = (ln & 15) >> 2, b_ = (ln >> 4) & 1;
      const unsigned bD = b_ * 8192 + (8 * (ln >> 5) + q_) * 128 + (((2 * ng) ^ ((q_ >> 1) | (b_ << 1))) * 32) + (ln & 3) * 8 + perm * 2048 + win;
#pragma unroll
      for (int e = 0; e < 2; ++e)
#pragma unroll
        for (int i = 0; i < 4; ++i) aD[e][i] = bD ^ (i * 2048 + e * 32);
    };
    init_aD(lane, 0);
    unsigned bX;
    {
      const int row = mt * 32 + (lane & 31);
      bX = (row * 128 + ((h ^ ((row >> 1) & 7)) * 16)) ^ (perm * 32);
    }
    auto tr2 = [&](unsigned a, unsigned off) AINL {
      const AS3 char* p = (const AS3 char*)a + off;
      return cat8(__builtin_amdgcn_ds_read_tr16_b64_v4bf16((AS3 bf16x4*)p), __builtin_amdgcn_ds_read_tr16_b64_v4bf16((AS3 bf16x4*)(p + 512)));
    };

    bf16x8 xf[32];
    bf16x8 wf[5];
    f32x16 A0, A1, B0, C0, C1;
    f32x4 cs0 = {0.f, 0.f, 0.f, 0.f}, cs1 = cs0, s1 = cs0;
    float s2 = 0.f, s2b = 0.f;
    bf16x8 pat;
    {
      const unsigned pv_ = (lane & 15) == 4 * (lane >> 4) ? 0x3f803f80u : 0u;
      const u32x4 t = {pv_, pv_, pv_, pv_};
      pat = __builtin_bit_cast(bf16x8, t);
    }
    auto rowsum = [&](f32x4& c, bf16x8 f) AINL { c = __builtin_amdgcn_mfma_f32_16x16x32_bf16(pat, f, c, 0, 0, 0); pin4(c); };
    auto sumsq = [&](int gs, int d) AINL {
      const unsigned v = __builtin_bit_cast(u32x4, xf[gs])[d];
      const float lo = __uint_as_float(v << 16), hi = __uint_as_float(v & 0xffff0000u);
      s2 = __builtin_fmaf(lo, lo, s2); pin(s2);
      s2b = __builtin_fmaf(hi, hi, s2b); pin(s2b);
    };
    const unsigned parta = L1_PART + ng * 2048 + l16;
    const int tt = rb * 4 + mt;

    float mneg, den, rg;
    f32x4 pp, su, sv;
    float te, tr_, tb;
    bf16x2 pa[4], pb[4];
    auto corr_rd = [&](int e) AINL { pp = lds_ld<f32x4>(parta + e * 1024); };
    auto corr = [&](f32x16& a) AINL {
      const float c = (pp[0] + pp[1]) + (pp[2] + pp[3]);
      const __bf16 hi = (__bf16)c;
      const float r1 = c - (float)hi;
      const __bf16 mid = (__bf16)r1;
      const __bf16 lo = (__bf16)(r1 - (float)mid);
      const __bf16 z = (__bf16)0.f, m = (__bf16)mneg;
      const bf16x4 fa = {hi, mid, lo, z}, fb = {m, m, m, m};
      a = __builtin_amdgcn_mfma_f32_32x32x8bf16_1k(__builtin_bit_cast(s16x4, fa), __builtin_bit_cast(s16x4, fb), a, 0, 0, 0);
      pin16(a);
    };
    auto epiA = [&](const f32x16& a, int j) AINL { te = __builtin_amdgcn_exp2f(a[j] * rg); pin(te); };
    auto epiB = [&]() AINL { tr_ = __builtin_amdgcn_rcpf(__builtin_fmaf(te, den, den)); pin(tr_); };
    auto epiC = [&](const f32x16& a, int j) AINL {
      const float vb = a[8 + j] * tr_;
      if (j & 1) { bf16x2 t = {(__bf16)tb, (__bf16)vb}; pb[j >> 1] = t; pin2(pb[j >> 1]); }
      else { tb = vb; pin(tb); }
    };
    auto pack8 = [&](const bf16x2* v) AINL {
      bf16x8 o = {v[0][0], v[0][1], v[1][0], v[1][1], v[2][0], v[2][1], v[3][0], v[3][1]};
      return o;
    };
    auto scale2 = [&](const f32x16& a, int j, float sc) AINL {
      bf16x2 t = {(__bf16)(a[j] * sc), (__bf16)(a[j + 1] * sc)}, u = {(__bf16)(a[8 + j] * sc), (__bf16)(a[9 + j] * sc)};
      pa[j >> 1] = t; pb[j >> 1] = u; pin2(pa[j >> 1]); pin2(pb[j >> 1]);
    };
    auto storeQKV = [&](int ga, int dh) AINL {
      const int which = ga >> 5, ha = ga & 31;
      const bf16x8 oa = pack8(pa), ob = pack8(pb);
      if (which < 2) {
        const unsigned tsz = which == 0 ? 1024 : 2048;
        const unsigned dst = (which == 0 ? WS_Q : WS_KV) + (ha * 64 + tt) * tsz;
        bst16(rws, l16, dst, oa); bst16(rws, l16, dst + dh * 64 * tsz, ob);
      } else {
        unsigned ln = l16; pinu(ln); ln >>= 4;
        const unsigned hh = ln >> 5, sb = L1_E0 + w * 2048;
        const unsigned wadr = sb + (ln & 31) * 64 + hh * 8;
        lds_st<bf16x4>(wadr, __builtin_shufflevector(oa, oa, 0, 1, 2, 3));
        lds_st<bf16x4>(wadr + 16, __builtin_shufflevector(oa, oa, 4, 5, 6, 7));
        lds_st<bf16x4>(wadr + 32, __builtin_shufflevector(ob, ob, 0, 1, 2, 3));
        lds_st<bf16x4>(wadr + 48, __builtin_shufflevector(ob, ob, 4, 5, 6, 7));
        const unsigned bk = (ln >> 4) & 1;
        const unsigned ra = sb + (8 * hh + ((ln & 15) >> 2)) * 64 + bk * 32 + (ln & 3) * 8;
        const unsigned dst = WS_KV + (ha * 64 + tt) * 2048 + 1024, vo = bk * dh * 64 * 2048 + hh * 256 + (ln & 15) * 16;
        bst16(rws, vo, dst, cat8(lds_tr(ra), lds_tr(ra + 256)));
        bst16(rws, vo, dst + 512, cat8(lds_tr(ra + 1024), lds_tr(ra + 1280)));
      }
    };
    auto side_job = [&](bool second) AINL {
      unsigned ln = l16; pinu(ln); ln >>= 4;
#pragma unroll
      for (int e = 0; e < 2; ++e) {
        if (e == 0 || w == 0) {
          const unsigned sj = L1_SJ + (e ? 8 : w) * 1024;
          if (!second) glds16s<false>(sjsrc[e], (ln >> 2) * (DM * 2) + (ln & 3) * 16, sj);
          else {
            const unsigned a = sj + (4 * (ln >> 5) + ((ln & 15) >> 2)) * 64 + ((ln >> 4) & 1) * 32 + (ln & 3) * 8;
            bst16(rws, l16, WS_WD + (blockIdx.x * 9 + (e ? 8 : w)) * 1024, cat8(lds_tr(a), lds_tr(a + 512)));
          }
        }
      }
    };

    sfor<0, 164>([&](auto ic) AINL {
      constexpr int hs = decltype(ic)::value, st = hs >> 3;
      if constexpr ((hs & 7) == 0 && st < 16) {
        if constexpr (st == 8) wait_barrier<k1_wait(st), 2>();
        else if constexpr (st == 9) wait_barrier<k1_wait(st), 6>();
        else if constexpr (st == 12) wait_barrier<k1_wait(st), 0>();
        else wait_barrier<k1_wait(st), 15>();
        if constexpr ((st & 3) == 0) STAMP(0, 1 + st / 4);
        if constexpr (st == 15) {
          if (w < 3 && (w == 0 || typeB) && l16 == 0) flag_st(ws + WS_F1 + ((w == 0 ? sngl : dblA + w - 1) * 16 + rb) * 8, epoch);
        }
      }
      SB0();
      if constexpr (hs >= 4) {
        constexpr int ms = hs - 4;
        if constexpr (ms < 64) {
          constexpr int mg = ms >> 1;
          f32x16& A = (ms & 1) ? A1 : A0;
          A = mfma(wf[ms % 5], xf[mg], mg == 0 ? zero16() : A); pin16(A);
          if constexpr ((mg & 3) == 0) rowsum((ms & 1) ? cs1 : cs0, wf[ms % 5]);
          if constexpr ((mg & 3) >= 2 && (ms & 1) == 0) rowsum(s1, xf[mg]);
        } else if constexpr (ms < 96) {
          B0 = mfma(wf[ms % 5], xf[ms - 64], ms == 64 ? zero16() : B0); pin16(B0);
          if constexpr ((ms & 3) == 0 && ms < 92) rowsum(cs0, wf[ms % 5]);
        } else {
          constexpr int mg = (ms - 96) >> 1;
          f32x16& C = (ms & 1) ? C1 : C0;
          C = mfma(wf[ms % 5], xf[mg], mg == 0 ? zero16() : C); pin16(C);
          if constexpr ((mg & 3) == 0) rowsum((ms & 1) ? cs1 : cs0, wf[ms % 5]);
        }
      }
      if constexpr (hs == 94) rowsum(cs0, wf[92 % 5]);
      if constexpr (st >= 1 && st < 8) sumsq(4 * (st - 1) + 2 + ((hs >> 2) & 1), hs & 3);
      if constexpr (hs >= 64 && hs < 68) { sumsq(30 + ((hs >> 1) & 1), 2 * (hs & 1)); sumsq(30 + ((hs >> 1) & 1), 2 * (hs & 1) + 1); }
      if constexpr (hs < 64) {
        constexpr int e = hs & 1, i = (hs >> 1) & 3, b = 2 * st;
        if constexpr (e == 0) xf[hs >> 1] = lds_ld<bf16x8>(vxor(bX, blk_slot(b + 1) + i * 32));
        wf[hs % 5] = tr2(aD[e][i], blk_slot(b) & 0xffffu);
        if constexpr ((st & 1) && st < 7) aD[e][i] = vxor(aD[e][i], 65536);
      } else if constexpr (hs < 96) {
        wf[hs % 5] = tr2(aS[hs & 3], ((hs - 64) >> 2) * 8192);
      } else if constexpr (hs < 160) {
        constexpr int e = hs & 1, i = (hs >> 1) & 3;
        wf[hs % 5] = tr2(aD[e][i], blk_slot(st + 8) & 0xffffu);
        if constexpr (st == 15) aD[e][i] = vxor(aD[e][i], 65536);
      }
      if constexpr (hs == 60) {
        unsigned ln = l16; pinu(ln); ln >>= 4;
        const unsigned q_ = (ln & 15) >> 2;
        const unsigned bS = (8 * (ln >> 5) + q_) * 128 + (((2 * ((ln >> 4) & 1) + ng) ^ (q_ >> 1)) * 32) + (ln & 3) * 8 + perm * 2048;
#pragma unroll
        for (int i = 0; i < 4; ++i) aS[i] = bS ^ (i * 2048);
      }
      if constexpr ((hs & 7) < 2) {
        if constexpr (st <= 9) load_blk(4 + 2 * st + (hs & 1));
        if constexpr (st >= 10 && st <= 13 && (hs & 1) == 0) load_blk(14 + st);
      }
      if constexpr (hs == 66) side_job(false);
      if constexpr (hs == 113) side_job(true);
      if constexpr (hs == 62 || hs == 158) {
        SB0();
        lds_st<float>(parta + mt * 4, cs0[0]); lds_st<float>(parta + 1024 + mt * 4, cs1[0]);
        cs0[0] = 0.f; cs1[0] = 0.f;
      }
      if constexpr (hs == 95) { SB0(); lds_st<float>(parta + mt * 4, cs0[0]); cs0[0] = 0.f; }
      if constexpr (hs == 66) corr_rd(0);
      if constexpr (hs == 68) {
        SB0();
        const unsigned sa = L1_E0 + mt * 1024 + (lane & 31) * 32 + (ng * 2 + h) * 4;
        lds_st<float>(sa, s1[0]); lds_st<float>(sa + 16, s2 + s2b);
      }
      if constexpr (hs == 72) {
        const unsigned sa = L1_E0 + mt * 1024 + (lane & 31) * 32;
        su = lds_ld<f32x4>(sa); sv = lds_ld<f32x4>(sa + 16);
      }
      if constexpr (hs == 74) {
        mneg = ((su[0] + su[1]) + (su[2] + su[3])) * (-1.0f / DM); pin(mneg);
        den = ((sv[0] + sv[1]) + (sv[2] + sv[3])) * (1.0f / DM); pin(den);
      }
      if constexpr (hs == 75) { den = bfround(bfround(fmaxf(den - mneg * mneg, 0.f)) + 1e-5f); pin(den); corr(A0); corr_rd(1); }
      if constexpr (hs == 76) { den = bfround(__builtin_amdgcn_sqrtf(den)); pin(den); }
      if constexpr (hs == 77) { rg = __builtin_amdgcn_rcpf(den) * -LOG2E; pin(rg); corr(A1); }
      if constexpr (hs == 80) {
        if (typeB) {
          const float sc = rg * (dblA < 8 ? -0.25f : -1.0f / LOG2E);
#pragma unroll
          for (int j = 0; j < 8; j += 2) scale2(A0, j, sc);
          storeQKV(dblA * 4 + 2 * ng, 4);
#pragma unroll
          for (int j = 0; j < 8; j += 2) scale2(A1, j, sc);
          storeQKV(dblA * 4 + 2 * ng + 1, 4);
        }
      }
      if constexpr (hs >= 78 && hs < 102) {
        constexpr int k = (hs - 78) / 3, ph = (hs - 78) % 3;
        if constexpr (ph == 0) epiA(A0, k);
        if constexpr (ph == 1) epiB();
        if constexpr (ph == 2) epiC(A0, k);
      }
      if constexpr (hs == 102) { if (!typeB) bst16(rws, l16, WS_H + (tt * 112 + dblA * 4 + 2 * ng) * 1024, pack8(pb)); }
      if constexpr (hs == 96) corr_rd(0);
      if constexpr (hs == 100) corr(B0);
      if constexpr (hs >= 103 && hs < 107) scale2(B0, 2 * (hs - 103), rg * (sngl < 8 ? -0.25f : -1.0f / LOG2E));
      if constexpr (hs == 107) storeQKV(sngl * 4 + ng, 2);
      if constexpr (hs >= 108 && hs < 132) {
        constexpr int k = (hs - 108) / 3, ph = (hs - 108) % 3;
        if constexpr (ph == 0) epiA(A1, k);
        if constexpr (ph == 1) epiB();
        if constexpr (ph == 2) epiC(A1, k);
      }
      if constexpr (hs == 132) { if (!typeB) bst16(rws, l16, WS_H + (tt * 112 + dblA * 4 + 2 * ng + 1) * 1024, pack8(pb)); }
    });
    SB0();
    STAMP(0, 6);

    const int head = bg * 4 + (bi >> 3), qb = bi & 7;
    unsigned lane1 = l16; pinu(lane1); lane1 >>= 4;
    const unsigned tid1 = w * 64 + lane1;
    if (w == 0) {
      const int u = lane1 < 16 ? (8 + bg) * 16 + lane1 : lane1 < 32 ? (16 + bg) * 16 + lane1 - 16 : bg * 16 + 2 * qb + (lane1 & 1);
      flag_wait(ws + WS_F1 + u * 8, epoch);
    }
    wait_barrier<63, 0>();
    STAMP(0, 7);
    const char* kvb = ws + WS_KV + (size_t)head * 131072 + w * 1024;
    const unsigned kvd = (w >> 1) * TB + (w & 1) * 1024;
    glds16s<true>(ws + WS_Q + (size_t)(head * 64 + qb * 8 + w) * 1024, l16, L2_Q + w * 1024);
#pragma unroll
    for (int i = 0; i < 3; ++i) glds16s<true>(kvb + i * 8192, l16, kvd + i * 4 * TB);
    corr_rd(0);
    const f32x4 pq = lds_ld<f32x4>(parta + 1024);
    if (tid1 < 132) {
      const unsigned v = (tid1 & 1) ? 0u : 0x3f803f80u;
      u32x4 c = {v, v, v, v};
      lds_st<u32x4>((tid1 >> 1) * TB + 2048 + (tid1 & 1) * 16, c);
    }
    corr(C0); pp = pq; corr(C1);
#pragma unroll
    for (int i = 3; i < 7; ++i) glds16s<true>(kvb + i * 8192, l16, kvd + i * 4 * TB);
    bf16x8 h0, h1;
#pragma unroll
    for (int j = 0; j < 8; ++j) {
      h0[j] = (__bf16)(C0[8 + j] * __builtin_amdgcn_rcpf(__builtin_fmaf(__builtin_amdgcn_exp2f(C0[j] * rg), den, den)));
      h1[j] = (__bf16)(C1[8 + j] * __builtin_amdgcn_rcpf(__builtin_fmaf(__builtin_amdgcn_exp2f(C1[j] * rg), den, den)));
    }
    bst16(rws, l16, WS_H + (tt * 112 + dblB * 4 + 2 * ng) * 1024, h0);
    bst16(rws, l16, WS_H + (tt * 112 + dblB * 4 + 2 * ng + 1) * 1024, h1);
  }

  const int rt = bg * 4 + (bi & 3), ct = bi >> 2;
  const unsigned hof = WS_H + (rt * 2 * 112 + w * 14) * 1024, wof = WS_WD + (ct * 2 * 112 + w * 14) * 1024;
  constexpr int NPF = 6;
  bf16x8 fh[14][2], fw[14][2];
  auto ld_down = [&](int s) AINL {
    fh[s][0] = bld16(rws, l16, hof + s * 1024); fh[s][1] = bld16(rws, l16, hof + (112 + s) * 1024);
    fw[s][0] = bld16(rws, l16, wof + s * 1024); fw[s][1] = bld16(rws, l16, wof + (112 + s) * 1024);
  };
  {
    const int head = bg * 4 + (bi >> 3), qt = (bi & 7) * 8 + w;
    const char* kvb = ws + WS_KV + (size_t)head * 131072 + w * 1024;
    const unsigned kvd = (w >> 1) * TB + (w & 1) * 1024;
    unsigned lane2 = l16; pinu(lane2); lane2 >>= 4;
    const int h = lane2 >> 5, d = lane2 & 31;
    const unsigned cslot = (d == 16 || d == 20) ? 2048 : 2064;
    const unsigned va0 = d < 16 ? 1024 + h * 256 + d * 16 : cslot;
    const unsigned va1 = d < 16 ? 1536 + h * 256 + d * 16 : cslot;
#define LDP(p, t) (*(const AS3 bf16x8*)((const AS3 char*)(p) + (t) * TB))
    wait_barrier<4, 0>();
    STAMP(0, 8);
    const bf16x8 q = lds_ld<bf16x8>(L2_Q + w * 1024 + l16);
    float m = xhalf_max(fmaxf(max16(mfma(LDP(l16, 0), q, zero16())), max16(mfma(LDP(l16, 1), q, zero16()))));
    f32x16 o, negm, s;
    bf16x8 kn, v0, v1, v1p;
    f32x8 ph;
    auto att_init = [&]() AINL {
#pragma unroll
      for (int i = 0; i < 16; ++i) negm[i] = -m;
      o = zero16();
      s = mfma(LDP(l16, 0), q, negm);
      kn = LDP(l16, 1); v0 = LDP(va0, 0); v1 = LDP(va1, 0);
#pragma unroll
      for (int i = 0; i < 8; ++i) ph[i] = 0.f;
      v1p = v1;
    };
    auto tiles4 = [&](int t0) AINL {
      const unsigned kp = l16 + t0 * TB, vp0 = va0 + t0 * TB, vp1 = va1 + t0 * TB;
#pragma unroll
      for (int u = 0; u < 4; ++u) {
        f32x16 sn = mfma(kn, q, negm);
        bf16x8 kn2 = LDP(kp, u + 2);
        bf16x8 v0n = LDP(vp0, u + 1), v1n = LDP(vp1, u + 1);
        f32x8 pl = exp8(lo8(s));
        o = pv(v1p, ph, o);
        ph = exp8(hi8(s));
        o = pv(v0, pl, o);
        v1p = v1; v0 = v0n; v1 = v1n; kn = kn2; s = sn;
        SGB(0x8, 1); SGB(0x100, 1); SGB(0x400, 5); SGB(0x8, 1); SGB(0x100, 1); SGB(0x400, 5); SGB(0x8, 1); SGB(0x100, 1); SGB(0x400, 6);
      }
    };
    att_init();
#pragma nounroll
    for (int t0 = 0; t0 < 32; t0 += 4) {
      if (t0 == 8) wait_barrier<6, 15>();
      if (t0 < 12) {
        const int i0 = 7 + (t0 >> 2) * 3;
#pragma unroll
        for (int i = 0; i < 3; ++i) glds16s<true>(kvb + (i0 + i) * 8192, l16, kvd + (i0 + i) * 4 * TB);
      }
      if (t0 == 24) {
        wait_barrier<0, 15>();
        if (w == 0 && l16 == 0) flag_st(ws + WS_F2 + blockIdx.x * 8, epoch);
      }
      tiles4(t0);
    }
    STAMP(0, 9);
#pragma unroll
    for (int i = 0; i < 4; ++i) flag_wait(ws + WS_F2 + i * 512 + (l16 >> 1), epoch);
    STAMP(0, 10);
    sfor<0, 8>([&](auto ic) AINL {
      constexpr int it = decltype(ic)::value;
      SB0();
      if constexpr (it < NPF) ld_down(it);
      SB0();
      tiles4(32 + 4 * it);
    });
    o = pv(v1p, ph, o);
    {
      float mx = o[8];
#pragma unroll
      for (int i = 0; i < 8; ++i) mx = fmaxf(mx, __builtin_fabsf(o[i]) * 0x1p-60f);
      bool ok = mx <= 0x1p60f;
#pragma unroll
      for (int i = 0; i <= 8; ++i) ok = ok && (o[i] == o[i]);
      if (!__all(ok)) {
        float tm = m;
#pragma nounroll
        for (int t = 0; t < 64; ++t) tm = fmaxf(tm, max16(mfma(LDP(l16, t), q, zero16())));
        m = xhalf_max(tm);
        att_init();
#pragma nounroll
        for (int t0 = 0; t0 < 64; t0 += 4) tiles4(t0);
        o = pv(v1p, ph, o);
      }
    }
    STAMP(0, 11);
    const float inv = 1.0f / o[8];
    bf16x8 ob;
#pragma unroll
    for (int j = 0; j < 8; ++j) ob[j] = (__bf16)(o[j] * inv);
    bst16(rws, l16, WS_O + (qt * 32 + head) * 1024, ob);

    f32x16 acc[4];
    auto mm_down = [&](int st) AINL {
      acc[0] = mfma(fw[st][0], fh[st][0], st == 0 ? zero16() : acc[0]);
      acc[1] = mfma(fw[st][0], fh[st][1], st == 0 ? zero16() : acc[1]);
      acc[2] = mfma(fw[st][1], fh[st][0], st == 0 ? zero16() : acc[2]);
      acc[3] = mfma(fw[st][1], fh[st][1], st == 0 ? zero16() : acc[3]);
      pin16(acc[0]); pin16(acc[1]); pin16(acc[2]); pin16(acc[3]);
    };
    SB0();
    mm_down(0); mm_down(1); mm_down(2);
    SB0();
    asm volatile("s_waitcnt vmcnt(0)" ::: "memory");
    if (l16 == 0) flag_st(ws + WS_F3 + (qt * 32 + head) * 8, epoch);
    SB0();
#pragma unroll
    for (int st = NPF; st < 12; ++st) ld_down(st);
    SB0();
#pragma unroll
    for (int st = 3; st < NPF; ++st) mm_down(st);
    SB0();
    ld_down(12); ld_down(13);
    SB0();
#pragma unroll
    for (int st = NPF; st < 10; ++st) mm_down(st);
    SB0();
    unsigned lane3 = l16; pinu(lane3); lane3 >>= 4;
    const int h3 = lane3 >> 5, d3 = lane3 & 31;
    const int mh = w & 1, kq = w >> 1;
    const unsigned oof = WS_O + ((rt * 2 + mh) * 32 + kq * 8) * 1024, pof = WS_WO + (ct * 2 * 32 + kq * 8) * 1024;
    bf16x8 fp[8][2], fo[8];
#pragma unroll
    for (int st = 0; st < 8; ++st) { fp[st][0] = bld16(rws, l16, pof + st * 1024); fp[st][1] = bld16(rws, l16, pof + (32 + st) * 1024); }
    const int row = w * 8 + (lane3 >> 3), cg = (lane3 & 7) * 8;
    const size_t eo = (size_t)(rt * 64 + row) * DM + ct * 64 + cg;
    const bf16x8 xv = *(const bf16x8*)(x + eo);
    SB0();
#pragma unroll
    for (int st = 10; st < 14; ++st) mm_down(st);
    SB0();
    STAMP(0, 12);
    const char* f3 = ws + WS_F3 + ((rt * 2 + mh) * 32 + kq * 8 + (lane3 & 7)) * 8;
    const bool ready = __all(flag_ld(f3) == epoch);
    asm volatile("" ::: "memory");
    if (ready) {
#pragma unroll
      for (int st = 0; st < 8; ++st) fo[st] = bld16(rws, l16, oof + st * 1024);
    }
    wait_barrier<63, 0>();
#pragma unroll
    for (int i = 0; i < 4; ++i) {
      const unsigned a = w * RPART + ((i & 1) * 32 + d3) * RROW + ((i >> 1) * 32 + 4 * h3) * 4;
#pragma unroll
      for (int g = 0; g < 4; ++g) {
        f32x4 t = {acc[i][4 * g], acc[i][4 * g + 1], acc[i][4 * g + 2], acc[i][4 * g + 3]};
        lds_st<f32x4>(a + g * 32, t);
      }
    }
    wait_barrier<63, 0>();
    float gg[8];
    {
      const unsigned a = row * RROW + cg * 4;
      f32x4 t0 = lds_ld<f32x4>(a), t1 = lds_ld<f32x4>(a + 16);
#pragma unroll
      for (int p = 1; p < 8; ++p) { t0 += lds_ld<f32x4>(a + p * RPART); t1 += lds_ld<f32x4>(a + p * RPART + 16); }
#pragma unroll
      for (int i = 0; i < 8; ++i) {
        const float z = i < 4 ? t0[i & 3] : t1[i & 3];
        gg[i] = z * __builtin_amdgcn_rcpf(1.0f + __builtin_amdgcn_exp2f(z * -LOG2E));
      }
    }
    STAMP(0, 13);
    if (!ready) {
      flag_wait(f3, epoch);
#pragma unroll
      for (int st = 0; st < 8; ++st) fo[st] = bld16(rws, l16, oof + st * 1024);
    }
    f32x16 c0 = zero16(), c1 = zero16();
#pragma unroll
    for (int st = 0; st < 8; ++st) { c0 = mfma(fp[st][0], fo[st], c0); c1 = mfma(fp[st][1], fo[st], c1); }
    STAMP(0, 14);
    wait_barrier<63, 0>();
    {
      const unsigned a = kq * RPART + (mh * 32 + d3) * RROW + 4 * h3 * 4;
#pragma unroll
      for (int g = 0; g < 4; ++g) {
        f32x4 t = {c0[4 * g], c0[4 * g + 1], c0[4 * g + 2], c0[4 * g + 3]}, u = {c1[4 * g], c1[4 * g + 1], c1[4 * g + 2], c1[4 * g + 3]};
        lds_st<f32x4>(a + g * 32, t); lds_st<f32x4>(a + 128 + g * 32, u);
      }
    }
    wait_barrier<63, 0>();
    {
      const unsigned a = row * RROW + cg * 4;
      f32x4 t0 = lds_ld<f32x4>(a), t1 = lds_ld<f32x4>(a + 16);
#pragma unroll
      for (int p = 1; p < 4; ++p) { t0 += lds_ld<f32x4>(a + p * RPART); t1 += lds_ld<f32x4>(a + p * RPART + 16); }
      bf16x8 ov;
#pragma unroll
      for (int i = 0; i < 8; ++i) ov[i] = (__bf16)__builtin_fmaf(i < 4 ? t0[i & 3] : t1[i & 3], gg[i], (float)xv[i]);
      *(bf16x8*)(out + eo) = ov;
    }
    if (blockIdx.x == 0 && w == 0 && l16 == 0) *(u64*)(ws + WS_F) = epoch;
    STAMP(0, 15); STAMP_FLUSH(0);
  }
}

}

extern "C" void kernel_launch(void* const* d_in, const int* in_sizes, int n_in, void* d_out, int out_size,
                              void* d_ws, size_t ws_size, hipStream_t stream) {
  (void)hipFuncSetAttribute((const void*)k_fused, hipFuncAttributeMaxDynamicSharedMemorySize, LDS_BYTES);
  k_fused<<<256, 512, LDS_BYTES, stream>>>((const __bf16*)d_in[0], (const __bf16*)d_in[1], (const __bf16*)d_in[4],
                                           (const __bf16*)d_in[5], (const __bf16*)d_in[6], (const __bf16*)d_in[2],
                                           (char*)d_ws, (__bf16*)d_out);
}
```

```cpp
#include <hip/hip_runtime.h>
#include <hip/hip_bf16.h>

typedef __attribute__((ext_vector_type(2))) __bf16 bf16x2;
typedef __attribute__((ext_vector_type(4))) __bf16 bf16x4;
typedef __attribute__((ext_vector_type(8))) __bf16 bf16x8;
typedef __attribute__((ext_vector_type(16))) __bf16 bf16x16;
typedef __attribute__((ext_vector_type(4))) float f32x4;
typedef __attribute__((ext_vector_type(8))) float f32x8;
typedef __attribute__((ext_vector_type(16))) float f32x16;
typedef __attribute__((ext_vector_type(4))) short s16x4;
typedef __attribute__((ext_vector_type(2))) unsigned u32x2;
typedef __attribute__((ext_vector_type(4))) unsigned u32x4;
#define AS3 __attribute__((address_space(3)))
#define DI __device__ __forceinline__

namespace {

constexpr int SEQ = 2048, DM = 512, FF = 1792;
constexpr size_t WS_Q = 0;
constexpr size_t WS_KV = WS_Q + 2097152;
constexpr size_t WS_H = WS_KV + 4194304;
constexpr size_t WS_WD = WS_H + 64 * 112 * 1024;
constexpr size_t WS_WO = WS_WD + 1792 * 1024;
constexpr size_t WS_O = WS_WO + 512 * 1024;
constexpr size_t WS_F = WS_O + 2097152;
constexpr size_t WS_F1 = WS_F + 1024, WS_F2 = WS_F + 4096, WS_ST = WS_F + 8192;

template <class T> DI T lds_ld(unsigned a) { return *(const AS3 T*)(a); }
template <class T> DI void lds_st(unsigned a, T v) { *(AS3 T*)(a) = v; }
DI bf16x4 lds_tr(unsigned a) { return __builtin_amdgcn_ds_read_tr16_b64_v4bf16((AS3 bf16x4*)(a)); }
DI bf16x8 cat8(bf16x4 a, bf16x4 b) { return __builtin_shufflevector(a, b, 0, 1, 2, 3, 4, 5, 6, 7); }
DI f32x16 mfma(bf16x8 a, bf16x8 b, f32x16 c) { return __builtin_amdgcn_mfma_f32_32x32x16_bf16(a, b, c, 0, 0, 0); }
DI f32x16 zero16() { f32x16 z; for (int i = 0; i < 16; ++i) z[i] = 0.f; return z; }
template <int N> DI void wait_vm() { asm volatile("s_waitcnt vmcnt(%0)" ::"i"(N) : "memory"); }
DI void pin(float& a) {
#if defined(__HIP_DEVICE_COMPILE__)
  asm volatile("" : "+v"(a));
#endif
}
DI void pin2(bf16x2& a) {
#if defined(__HIP_DEVICE_COMPILE__)
  asm volatile("" : "+v"(a));
#endif
}
DI void pin4(f32x4& a) {
#if defined(__HIP_DEVICE_COMPILE__)
  asm volatile("" : "+v"(a));
#endif
}
DI void pinb4(bf16x4& a) {
#if defined(__HIP_DEVICE_COMPILE__)
  asm volatile("" : "+v"(a));
#endif
}
DI void pinb8(bf16x8& a) {
#if defined(__HIP_DEVICE_COMPILE__)
  asm volatile("" : "+v"(a));
#endif
}
DI void pin8(f32x8& a) {
#if defined(__HIP_DEVICE_COMPILE__)
  asm volatile("" : "+v"(a));
#endif
}
DI void pin16(f32x16& a) {
#if defined(__HIP_DEVICE_COMPILE__)
  asm volatile("" : "+v"(a));
#endif
}
template <int V> struct IC { static constexpr int value = V; };
template <int B, int E, class F> DI void sfor(F&& f) {
  if constexpr (B < E) { f(IC<B>{}); sfor<B + 1, E>(f); }
}
#define AINL __attribute__((always_inline))
#define SB0() __builtin_amdgcn_sched_barrier(0)
DI unsigned vxor(unsigned a, unsigned c) {
  unsigned r = 0;
#if defined(__HIP_DEVICE_COMPILE__)
  asm volatile("v_xor_b32 %0, %1, %2" : "=v"(r) : "i"(c), "v"(a));
#endif
  return r;
}
DI float bfround(float x) { return (float)(__bf16)x; }
DI float row_sum16(float v) {
  v += __builtin_bit_cast(float, __builtin_amdgcn_update_dpp(0, __builtin_bit_cast(int, v), 0x128, 0xf, 0xf, false));
  v += __builtin_bit_cast(float, __builtin_amdgcn_update_dpp(0, __builtin_bit_cast(int, v), 0x124, 0xf, 0xf, false));
  v += __builtin_bit_cast(float, __builtin_amdgcn_update_dpp(0, __builtin_bit_cast(int, v), 0x122, 0xf, 0xf, false));
  v += __builtin_bit_cast(float, __builtin_amdgcn_update_dpp(0, __builtin_bit_cast(int, v), 0x121, 0xf, 0xf, false));
  return v;
}
DI float rows_sum(float v) {
  const int i = __builtin_bit_cast(int, v);
  return (__builtin_bit_cast(float, __builtin_amdgcn_readlane(i, 0)) + __builtin_bit_cast(float, __builtin_amdgcn_readlane(i, 16))) +
         (__builtin_bit_cast(float, __builtin_amdgcn_readlane(i, 32)) + __builtin_bit_cast(float, __builtin_amdgcn_readlane(i, 48)));
}
DI float xhalf_max(float v) {
  auto r = __builtin_amdgcn_permlane32_swap(__float_as_uint(v), __float_as_uint(v), false, false);
  return fmaxf(__uint_as_float(r[0]), __uint_as_float(r[1]));
}
typedef __amdgpu_buffer_rsrc_t rsrc_t;
DI rsrc_t mk_rsrc(const void* p, unsigned bytes) { return __builtin_amdgcn_make_buffer_rsrc((void*)p, 0, bytes, 0x00020000); }
#define ST_AUX 16
#define LD_AUX 16
DI void bst16(rsrc_t r, unsigned voff, unsigned soff, bf16x8 v) {
  __builtin_amdgcn_sched_barrier(0);
  __builtin_amdgcn_raw_buffer_store_b128(__builtin_bit_cast(u32x4, v), r, voff, soff, ST_AUX);
  asm volatile("s_nop 1" ::: "memory");
  __builtin_amdgcn_sched_barrier(0);
}
template <int AUX = LD_AUX> DI bf16x8 bld16(rsrc_t r, unsigned voff, unsigned soff) {
  return __builtin_bit_cast(bf16x8, __builtin_amdgcn_raw_buffer_load_b128(r, voff, soff, AUX));
}
DI void acquire() { asm volatile("" ::: "memory"); }

constexpr int att_prio(int it) { return 3 - (it + 2) % 3; }
constexpr int FPOLL = 150;
constexpr unsigned SL = 16384;
constexpr unsigned L1_E0 = 8 * SL, L1_E1 = 9 * SL;
constexpr unsigned L1_SJ = L1_E1;
constexpr unsigned L1_PART = L1_E1 + 9216;
constexpr unsigned LDS_BYTES = 163840;
constexpr unsigned blk_slot(int b) { return b == 16 ? L1_E0 : ((b + 4) & 7) * SL; }
constexpr int PUB = 15;
constexpr int WC_LANES = 3;
constexpr int k1_wait(int st) { return (st == 1 || st == 3 || st == 4) ? 5 : st == 6 ? 6 : st <= 7 ? 4 : st <= 9 ? 7 : st == 10 ? 8 : st <= 14 ? 10 : 0; }
constexpr unsigned TB = 2080;
constexpr unsigned L2_Q = 66 * TB;
constexpr unsigned L2_F = L2_Q + 24576;
constexpr unsigned SPIN_CAP = 1u << 18;

template <int V, int L> DI void wait_barrier() {
  asm volatile("s_waitcnt vmcnt(%0) lgkmcnt(%1)\n\ts_barrier" ::"i"(V), "i"(L) : "memory");
}
template <bool SC1> DI void glds16s(const void* sbase, unsigned voff, unsigned lds_dst) {
#if defined(__HIP_DEVICE_COMPILE__)
  if (SC1) asm volatile("s_mov_b32 m0, %2\n\ts_nop 0\n\tglobal_load_lds_dwordx4 %0, %1 sc1" ::"v"(voff), "s"(sbase), "s"(lds_dst) : "memory");
  else asm volatile("s_mov_b32 m0, %2\n\ts_nop 0\n\tglobal_load_lds_dwordx4 %0, %1" ::"v"(voff), "s"(sbase), "s"(lds_dst) : "memory");
#endif
}
DI u32x4 mk_srd(const void* p, unsigned bytes) {
  const unsigned long long a = (unsigned long long)p;
  u32x4 r = {(unsigned)a, (unsigned)(a >> 32) & 0xffffu, bytes, 0x00020000u};
  return r;
}
DI void dma_dst(unsigned base, unsigned c) {
#if defined(__HIP_DEVICE_COMPILE__)
  asm volatile("s_add_i32 m0, %0, %1" ::"s"(base), "i"(c) : "memory", "scc");
#endif
}
DI void dma_go(u32x4 srd, unsigned voff, unsigned soff) {
#if defined(__HIP_DEVICE_COMPILE__)
  asm volatile("buffer_load_dwordx4 %0, %1, %2 offen lds" ::"v"(voff), "s"(srd), "s"(soff) : "memory");
#endif
}
DI void pinu(unsigned& a) {
#if defined(__HIP_DEVICE_COMPILE__)
  asm volatile("" : "+v"(a));
#endif
}
typedef unsigned long long u64;
typedef __attribute__((address_space(1))) u64 gu64;
DI u64 flag_ld(const char* p) { return __hip_atomic_load((gu64*)p, __ATOMIC_RELAXED, __HIP_MEMORY_SCOPE_AGENT); }
DI void flag_st(char* p, u64 v) { __hip_atomic_store((gu64*)p, v, __ATOMIC_RELAXED, __HIP_MEMORY_SCOPE_AGENT); }
DI void flag_wait(const char* p, u64 epoch) {
  for (unsigned spins = 0; spins < SPIN_CAP; ++spins) {
    if (__all(flag_ld(p) == epoch)) break;
    __builtin_amdgcn_s_sleep(2);
  }
  asm volatile("" ::: "memory");
}
DI f32x16 pv(bf16x8 v, f32x8 p, f32x16 o) {
  return __builtin_amdgcn_smfmac_f32_32x32x32_bf16(v, __builtin_bit_cast(bf16x16, p), o, 0xDDDD, 0, 0);
}
DI f32x8 exp8(f32x8 s) { f32x8 r; for (int i = 0; i < 8; ++i) r[i] = __builtin_amdgcn_exp2f(s[i]); return r; }
DI f32x8 lo8(f32x16 s) { return __builtin_shufflevector(s, s, 0, 1, 2, 3, 4, 5, 6, 7); }
DI f32x8 hi8(f32x16 s) { return __builtin_shufflevector(s, s, 8, 9, 10, 11, 12, 13, 14, 15); }
DI float max16(f32x16 s) {
  float m = fmaxf(s[0], s[1]);
  for (int i = 2; i < 16; i += 2) m = fmaxf(m, fmaxf(s[i], s[i + 1]));
  return m;
}
DI unsigned lds_add(unsigned a, unsigned l16) {
  unsigned r = 0;
  asm volatile("" ::: "memory");
  if (l16 == 0) r = __hip_atomic_fetch_add((AS3 unsigned*)a, 1u, __ATOMIC_RELAXED, __HIP_MEMORY_SCOPE_WORKGROUP);
  return __builtin_amdgcn_readfirstlane(r);
}
DI void lds_wait_ge(unsigned a, unsigned n) {
  while (__builtin_amdgcn_readfirstlane(*(volatile AS3 unsigned*)a) < n) __builtin_amdgcn_s_sleep(1);
  asm volatile("" ::: "memory");
}
#define SGB(m, n) __builtin_amdgcn_sched_group_barrier(m, n, 0)

__global__ __launch_bounds__(512, 2) void k_fused(const __bf16* __restrict__ x, const __bf16* __restrict__ wqkv,
                                                  const __bf16* __restrict__ wg, const __bf16* __restrict__ wu,
                                                  const __bf16* __restrict__ wd, const __bf16* __restrict__ wo,
                                                  char* __restrict__ ws, __bf16* __restrict__ out) {
  const int tid = threadIdx.x, lane = tid & 63;
  const int w = __builtin_amdgcn_readfirstlane(tid >> 6);
  const int bg = blockIdx.x & 7, bi = blockIdx.x >> 3;
  unsigned l16_ = lane * 16; pinu(l16_);
  const unsigned l16 = l16_;
  const rsrc_t rws = mk_rsrc(ws, (unsigned)WS_F);
  constexpr float LOG2E = 1.4426950408889634f;
  u64 epoch = 0;
#if defined(__HIP_DEVICE_COMPILE__)
  asm volatile("" ::"s"(wd), "s"(wo), "s"(out), "s"(x), "s"(ws));
#endif

  bf16x8 hA1, sj9;
  unsigned hA1o;
  {
    const int mt = w & 3, ng = w >> 2;
    const int perm = mt ^ (2 * ng);
    const int rb = (bg & 1) * 8 + (bi & 7), grp = bg >> 1, cj = bi >> 3;
    const bool typeB = cj == 3;
    const int dblB = 7 * grp + 2 * cj;
    const int dblA = typeB ? (grp >> 1) * 8 + (grp & 1) * 2 : dblB + 1;
    const int sk = ((cj + 1) & 3) * 4 + grp;
    const int sngl = sk < 4 ? sk + 4 : sk + 8;
    const int rot = (rb + 2 * cj) & 7;
    const int h = lane >> 5;

    constexpr unsigned SG = FF * 2, SQ = 3 * DM * 2;
    const int r8 = lane >> 3, c8 = lane & 7;
    const unsigned vcol = (((c8 >> 1) ^ ((r8 >> 1) & 1)) * 32) + (c8 & 1) * 16;
    const unsigned vG = (w * 8 + r8) * SG + vcol + dblB * 128, vQ0 = (w * 8 + r8) * SQ + vcol;
    const unsigned vS = vQ0 + sngl * 128;
    const unsigned vX0 = (rb * 128 + w * 8 + r8) * (DM * 2) + ((c8 ^ ((w * 4 + (r8 >> 1)) & 7)) * 16), vX1 = vX0 + 64 * DM * 2;
    const unsigned vA0 = typeB ? vQ0 + dblA * 128 : vG + 128, vA1 = (typeB ? vA0 + 128 : vA0) ^ 64;
    const u32x4 dX = mk_srd(x, SEQ * DM * 2), dQ = mk_srd(wqkv, DM * SQ), dG = mk_srd(wg, DM * SG), dU = mk_srd(wu, DM * SG);
    const u32x4 dA0 = typeB ? dQ : dG, dA1 = typeB ? dQ : dU;
    const unsigned hstrA = (typeB ? SQ : SG) / 2, w1k = w * 1024;
    auto kc128 = [&](int j) AINL { return (unsigned)((j + rot) & 7) * 128u; };
    auto half_dst = [&](int b, int hf) AINL { dma_dst(w1k, blk_slot(b) + hf * 8192); };
    auto half_go = [&](int b, int hf) AINL {
      if (b < 16) {
        if (b & 1) dma_go(dX, hf ? vX1 : vX0, kc128(b >> 1));
        else dma_go(hf ? dA1 : dA0, hf ? vA1 : vA0, kc128(b >> 1) * hstrA);
      } else if (b < 20) dma_go(dQ, vS, kc128(2 * (b - 16) + hf) * (SQ / 2));
      else dma_go(hf ? dU : dG, hf ? vG ^ 64 : vG, kc128(b - 20) * (SG / 2));
    };
    u32x4 xrow = {0, 0, 0, 0};
    const int srow = rb * 128 + (grp * 4 + cj) * 8 + w;
    sfor<0, 4>([&](auto ic) AINL {
      constexpr int i = decltype(ic)::value;
      half_dst(i >> 1, i & 1);
      asm volatile("s_nop 0" ::: "memory");
      half_go(i >> 1, i & 1);
    });
#if defined(__HIP_DEVICE_COMPILE__)
    asm volatile("s_load_dwordx2 %0, %1, 0x0" : "=s"(epoch) : "s"(ws + WS_F) : "memory");
#endif
    if (tid < 64) lds_st<unsigned>(L2_F + tid * 4, 0u);
    const char* sjsrc[2];
#pragma unroll
    for (int e = 0; e < 2; ++e) {
      const int f = blockIdx.x * 9 + (e ? 8 : w);
      const bool isd = f < 1792;
      const int f2 = isd ? f : f - 1792;
      const int nt = isd ? f2 / 112 : f2 >> 5;
      const int ks = isd ? f2 - nt * 112 : f2 & 31;
      sjsrc[e] = (const char*)(isd ? wd : wo) + ks * 16 * (DM * 2) + nt * 64;
    }

    const int q4 = (lane & 15) >> 2, p4 = lane & 3, blk = (lane >> 4) & 1;
    unsigned aD[2][4];
    unsigned aS[4];
    auto init_aD = [&](unsigned ln, unsigned win) AINL {
      const unsigned q_ = (ln & 15) >> 2, b_ = (ln >> 4) & 1;
      const unsigned bD = b_ * 8192 + (8 * (ln >> 5) + q_) * 128 + (((2 * ng) ^ ((q_ >> 1) | (b_ << 1))) * 32) + (ln & 3) * 8 + perm * 2048 + win;
#pragma unroll
      for (int e = 0; e < 2; ++e)
#pragma unroll
        for (int i = 0; i < 4; ++i) aD[e][i] = bD ^ (i * 2048 + e * 32);
    };
    init_aD(lane, 65536);
    {
      const unsigned bS = (8 * h + q4) * 128 + (((2 * blk + ng) ^ (q4 >> 1)) * 32) + p4 * 8 + perm * 2048 + L1_E0;
#pragma unroll
      for (int i = 0; i < 4; ++i) { aS[i] = bS ^ (i * 2048); pinu(aS[i]); }
    }
    unsigned bX;
    {
      const int row = mt * 32 + (lane & 31);
      bX = (row * 128 + ((h ^ ((row >> 1) & 7)) * 16)) ^ (perm * 32);
    }
    auto tr2 = [&](unsigned a, unsigned off) AINL {
      const AS3 char* p = (const AS3 char*)a + off;
      return cat8(__builtin_amdgcn_ds_read_tr16_b64_v4bf16((AS3 bf16x4*)p), __builtin_amdgcn_ds_read_tr16_b64_v4bf16((AS3 bf16x4*)(p + 512)));
    };

    bf16x8 xf[32];
    bf16x8 wf[5];
    f32x16 A0, A1, B0, C0, C1;
    f32x4 cs0 = {0.f, 0.f, 0.f, 0.f}, cs1 = cs0;
    bf16x8 pat;
    {
      const unsigned pv_ = (lane & 15) == 4 * (lane >> 4) ? 0x3f803f80u : 0u;
      const u32x4 t = {pv_, pv_, pv_, pv_};
      pat = __builtin_bit_cast(bf16x8, t);
    }
    auto rowsum = [&](f32x4& c, bf16x8 f) AINL { c = __builtin_amdgcn_mfma_f32_16x16x32_bf16(pat, f, c, 0, 0, 0); pin4(c); };
    const unsigned parta = L1_PART + ng * 2048 + l16;
    const int tt = rb * 4 + mt;

    float mneg, den, rg;
    f32x4 pp, pq;
    u64 stg = 0;
    f32x8 xe;
    float mu, sq;
    float te, tr_, tb;
    bf16x2 pa[4], pb[4];
    auto corr_rd = [&](int e) AINL { pp = lds_ld<f32x4>(parta + e * 1024); };
    float cc, cr;
    auto corr1 = [&](const f32x4& p) AINL {
      cc = (p[0] + p[1]) + (p[2] + p[3]);
      cr = cc - (float)(__bf16)cc;
      pin(cc); pin(cr);
    };
    bf16x4 fa0, fa1;
    auto corr_frag = [&](bf16x4& fa) AINL {
      const __bf16 mid = (__bf16)cr;
      const bf16x4 t = {(__bf16)cc, mid, (__bf16)(cr - (float)mid), (__bf16)0.f};
      fa = t; pinb4(fa);
    };
    auto corr_go = [&](f32x16& a, const bf16x4& fa) AINL {
      const __bf16 m = (__bf16)mneg;
      const bf16x4 fb = {m, m, m, m};
      a = __builtin_amdgcn_mfma_f32_32x32x8bf16_1k(__builtin_bit_cast(s16x4, fa), __builtin_bit_cast(s16x4, fb), a, 0, 0, 0);
      pin16(a);
    };
    auto corr2 = [&](f32x16& a) AINL { corr_frag(fa0); corr_go(a, fa0); };
    auto epiA = [&](const f32x16& a, int j) AINL { te = __builtin_amdgcn_exp2f(a[j] * rg); pin(te); };
    auto epiB = [&]() AINL { tr_ = __builtin_amdgcn_rcpf(__builtin_fmaf(te, den, den)); pin(tr_); };
    auto epiC = [&](const f32x16& a, int j) AINL {
      const float vb = a[8 + j] * tr_;
      if (j & 1) { bf16x2 t = {(__bf16)tb, (__bf16)vb}; pb[j >> 1] = t; pin2(pb[j >> 1]); }
      else { tb = vb; pin(tb); }
    };
    auto pack8 = [&](const bf16x2* v) AINL {
      bf16x8 o = {v[0][0], v[0][1], v[1][0], v[1][1], v[2][0], v[2][1], v[3][0], v[3][1]};
      return o;
    };
    auto scale2 = [&](const f32x16& a, int j, float sc) AINL {
      bf16x2 t = {(__bf16)(a[j] * sc), (__bf16)(a[j + 1] * sc)}, u = {(__bf16)(a[8 + j] * sc), (__bf16)(a[9 + j] * sc)};
      pa[j >> 1] = t; pb[j >> 1] = u; pin2(pa[j >> 1]); pin2(pb[j >> 1]);
    };
    auto storeQK = [&](int ga, int dh) AINL {
      const unsigned tsz = ga < 32 ? 1024 : 2048;
      const unsigned dst = (ga < 32 ? WS_Q : WS_KV) + ((ga & 31) * 64 + tt) * tsz;
      bst16(rws, l16, dst, pack8(pa)); bst16(rws, l16, dst + dh * 64 * tsz, pack8(pb));
    };
    bf16x8 vt0, vt1;
    auto storeV = [&](int ga, int part) AINL {
      unsigned ln = l16; pinu(ln); ln >>= 4;
      const unsigned hh = ln >> 5, sb = L1_E0 + w * 2048, bk = (ln >> 4) & 1, q_ = (ln & 15) >> 2;
      if (part == 0) {
        const unsigned wadr = sb + (ln & 31) * 64 + ((ln >> 1) & 3) * 16 + ((hh ^ (ln >> 3)) & 1) * 8;
        const bf16x8 oa = pack8(pa), ob = pack8(pb);
        lds_st<bf16x4>(wadr, __builtin_shufflevector(oa, oa, 0, 1, 2, 3));
        lds_st<bf16x4>(wadr ^ 16, __builtin_shufflevector(oa, oa, 4, 5, 6, 7));
        lds_st<bf16x4>(wadr ^ 32, __builtin_shufflevector(ob, ob, 0, 1, 2, 3));
        lds_st<bf16x4>(wadr ^ 48, __builtin_shufflevector(ob, ob, 4, 5, 6, 7));
        const unsigned ra = sb + (8 * hh + q_) * 64 + (((bk * 2 + ((ln >> 1) & 1)) ^ (q_ >> 1)) * 16) + (((ln ^ hh) & 1) * 8), rb_ = (ra ^ 32) + 256;
        vt0 = cat8(lds_tr(ra), lds_tr(rb_));
        vt1 = cat8(lds_tr(ra + 1024), lds_tr(rb_ + 1024));
      } else {
        const unsigned dst = WS_KV + ((ga & 31) * 64 + tt) * 2048 + 1024, vo = bk * 2 * 64 * 2048 + hh * 256 + (ln & 15) * 16;
        bst16(rws, vo, dst, vt0);
        bst16(rws, vo, dst + 512, vt1);
      }
    };
    auto side_job = [&](bool second, int e0 = 0, int e1 = 2) AINL {
      unsigned ln = l16; pinu(ln); ln >>= 4;
#pragma unroll
      for (int e = e0; e < e1; ++e) {
        if (e == 0 || w == 0) {
          const unsigned sj = L1_SJ + (e ? 8 : w) * 1024;
          if (!second) glds16s<false>(sjsrc[e], (ln >> 2) * (DM * 2) + (ln & 3) * 16, sj);
          else {
            const unsigned a = sj + (4 * (ln >> 5) + ((ln & 15) >> 2)) * 64 + ((ln >> 4) & 1) * 32 + (ln & 3) * 8;
            bst16(rws, l16, WS_WD + (blockIdx.x * 9 + (e ? 8 : w)) * 1024, cat8(lds_tr(a), lds_tr(a + 512)));
          }
        }
      }
    };

    const int head = bg * 4 + (bi >> 3), qb = bi & 7;
    const char* kvb = ws + WS_KV + (size_t)head * 131072 + w * 1024;
    const unsigned kvd = (w >> 1) * TB + (w & 1) * 1024;
    auto kv_first = [&](int i) AINL {
      if (i == 0) glds16s<true>(ws + WS_Q + (size_t)(head * 64 + qb * 8 + w) * 1024, l16, L2_Q + w * 1024);
      else glds16s<true>(kvb + (i - 1) * 8192, l16, kvd + (i - 1) * 4 * TB);
    };
    u64 f1v = 0;
    bool early = false;
    u64 f1w = 0;
    unsigned f1o = 0;

#pragma unroll
    for (int i = 0; i < 8; ++i) pinu(aD[i >> 2][i & 3]);
    pinu(bX); pinb8(pat);
#if defined(__HIP_DEVICE_COMPILE__)
    asm volatile("" ::"s"(sjsrc[0]), "s"(sjsrc[1]));
#endif
    SB0();
#if defined(__HIP_DEVICE_COMPILE__)
    asm volatile("buffer_load_dwordx4 %0, %1, %2, %3 offen" : "=v"(xrow) : "v"(l16), "s"(dX), "s"(srow * (DM * 2)) : "memory");
#endif
    sfor<4, 8>([&](auto ic) AINL {
      constexpr int i = decltype(ic)::value;
      half_dst(i >> 1, i & 1);
      asm volatile("s_nop 0" ::: "memory");
      half_go(i >> 1, i & 1);
    });
    {
#if defined(__HIP_DEVICE_COMPILE__)
      unsigned long long pc;
      unsigned len;
      asm volatile("s_getpc_b64 %0\n.Lfz_here:\n\ts_mov_b32 %1, .Lfz_end - .Lfz_here" : "=s"(pc), "=s"(len));
      const u32x4 dC = mk_srd((const void*)pc, len);
      asm volatile("s_mov_b32 m0, %2\n\ts_nop 0\n\tbuffer_load_dword %0, %1, 0 offen lds" ::"v"(lane < WC_LANES ? (((bi & 15) * 8 + w) * WC_LANES + lane) * 128u : 0x7fffff00u), "s"(dC), "s"(L1_SJ + w * 256) : "memory");
#endif
    }

    sfor<0, 164>([&](auto ic) AINL {
      constexpr int hs = decltype(ic)::value, st = hs >> 3, p = hs & 7;
      if constexpr (hs == 0) {
#if defined(__HIP_DEVICE_COMPILE__)
        asm volatile("s_waitcnt vmcnt(6) lgkmcnt(0)\n\ts_barrier" : "+s"(epoch)::"memory");
#endif
        epoch += 0x9E3779B97F4A7C15ull;
      } else if constexpr (p == 0 && st <= PUB) {
        wait_barrier<k1_wait(st), (hs == 64 || hs == 96) ? 2 : 15>();
        if constexpr (st == PUB) {
          if (w < 3 && (w == 0 || typeB) && l16 == 0) flag_st(ws + WS_F1 + ((w == 0 ? sngl : dblA + w - 1) * 16 + rb) * 8, epoch);
        }
      }
      if constexpr (hs == 158) wait_barrier<63, 4>();
      SB0();
      if constexpr (hs >= 4) {
        constexpr int ms = hs - 4;
        if constexpr (ms < 64) {
          constexpr int mg = ms >> 1;
          f32x16& A = (ms & 1) ? A1 : A0;
          A = mfma(wf[ms % 5], xf[mg], mg == 0 ? zero16() : A); pin16(A);
        } else if constexpr (ms < 96) {
          B0 = mfma(wf[ms % 5], xf[ms - 64], ms == 64 ? zero16() : B0); pin16(B0);
        } else {
          constexpr int mg = (ms - 96) >> 1;
          f32x16& C = (ms & 1) ? C1 : C0;
          C = mfma(wf[ms % 5], xf[mg], mg == 0 ? zero16() : C); pin16(C);
        }
      }
      if constexpr (hs == 63 || hs == 156) {
        lds_st<float>(parta + mt * 4, cs0[0]); lds_st<float>(parta + 1024 + mt * 4, cs1[0]);
        cs0[0] = 0.f; cs1[0] = 0.f;
        SB0();
      }
      if constexpr (hs == 95) { lds_st<float>(parta + mt * 4, cs0[0]); cs0[0] = 0.f; SB0(); }
      constexpr int db = st == 5 ? (p < 6 ? 14 + (p >> 1) : -1) : st == 6 ? (p < 2 ? 17 : -1) : st <= 7 ? (p < 4 ? 4 + 2 * st + (p >> 1) : -1) : (p & 1) ? -1 : st == 8 ? (p >= 4 ? 20 : -1) : st <= 10 ? 3 + 2 * st + (p >> 2) : st <= 13 ? (p < 4 ? 14 + st : -1) : -1;
      constexpr int dh = st <= 7 ? (p & 1) : ((p >> 1) & 1);
      if constexpr (db >= 0) half_dst(db, dh);
      if constexpr (hs < 64) {
        constexpr int e = hs & 1, i = p >> 1, b = 2 * st;
        if constexpr (e == 0) xf[hs >> 1] = lds_ld<bf16x8>(vxor(bX, blk_slot(b + 1) + i * 32));
        wf[hs % 5] = tr2(aD[e][i], blk_slot(b) & 0xffffu);
        if constexpr ((st & 1) && st < 7) aD[e][i] = vxor(aD[e][i], 65536);
      } else if constexpr (hs < 96) {
        wf[hs % 5] = tr2(aS[hs & 3], ((hs - 64) >> 2) * 8192);
        if constexpr (hs >= 68 && hs < 72) aS[hs & 3] = vxor(aS[hs & 3], L1_E0 ^ 65536);
      } else if constexpr (hs < 160) {
        constexpr int e = hs & 1, i = p >> 1;
        wf[hs % 5] = tr2(aD[e][i], blk_slot(st + 8) & 0xffffu);
        if constexpr (st == 15) aD[e][i] = vxor(aD[e][i], 65536);
      }
      if constexpr (db >= 0) half_go(db, dh);
      if constexpr (hs >= 4) {
        constexpr int ms = hs - 4;
        SB0();
        if constexpr (hs >= 152) { if constexpr (hs == 154 || hs == 155) rowsum((p & 1) ? cs1 : cs0, wf[(hs - 2) % 5]); }
        else if constexpr (ms < 64 || ms >= 96) { if constexpr (p == 4 || p == 5) rowsum((p & 1) ? cs1 : cs0, wf[ms % 5]); }
        else {
          if constexpr ((ms & 3) == 0 && ms < 92) rowsum(cs0, wf[ms % 5]);
          if constexpr (ms == 90) rowsum(cs0, wf[92 % 5]);
        }
      }
      if constexpr (hs == 8 * PUB + 2) side_job(false);
      if constexpr (hs == 9) {
#if defined(__HIP_DEVICE_COMPILE__)
        asm volatile("" : "+v"(xrow));
#endif
#pragma unroll
        for (int i = 0; i < 4; ++i) { xe[2 * i] = __uint_as_float(xrow[i] << 16); xe[2 * i + 1] = __uint_as_float(xrow[i] & 0xffff0000u); }
        pin8(xe);
      }
      if constexpr (hs == 10) { mu = ((xe[0] + xe[1]) + (xe[2] + xe[3])) + ((xe[4] + xe[5]) + (xe[6] + xe[7])); pin(mu); }
      if constexpr (hs == 11) { mu = row_sum16(mu); pin(mu); }
      if constexpr (hs == 12) { mu = rows_sum(mu) * (1.0f / DM); pin(mu); }
      if constexpr (hs == 13) { xe -= mu; pin8(xe); }
      if constexpr (hs == 14) { sq = (xe[0] * xe[0] + xe[1] * xe[1]) + (xe[2] * xe[2] + xe[3] * xe[3]); pin(sq); }
      if constexpr (hs == 15) { sq += (xe[4] * xe[4] + xe[5] * xe[5]) + (xe[6] * xe[6] + xe[7] * xe[7]); pin(sq); }
      if constexpr (hs == 16) { sq = row_sum16(sq); pin(sq); }
      if constexpr (hs == 17) { sq = bfround(rows_sum(sq) * (1.0f / DM)); pin(sq); }
      if constexpr (hs == 18) { sq = __builtin_amdgcn_sqrtf(bfround(sq + 1e-5f)); pin(sq); }
      if constexpr (hs == 20) {
        const bf16x2 ms = {(__bf16)mu, (__bf16)sq};
        if (l16 == 0) flag_st(ws + WS_ST + srow * 8, (epoch << 32) | __builtin_bit_cast(unsigned, ms));
      }
      if constexpr (hs == 60) {
#if defined(__HIP_DEVICE_COMPILE__)
        asm volatile("global_load_dwordx2 %0, %1, %2 sc1" : "=v"(stg) : "v"((l16 >> 1) & 0xf8), "s"(ws + WS_ST + tt * 256) : "memory");
#endif
      }
      if constexpr (hs == 64) { corr_rd(0); pq = lds_ld<f32x4>(parta + 1024); }
      if constexpr (hs == 68) corr1(pp);
      if constexpr (hs == 69) corr_frag(fa0);
      if constexpr (hs == 70) corr1(pq);
      if constexpr (hs == 71) corr_frag(fa1);
      if constexpr (hs == 75) {
#if defined(__HIP_DEVICE_COMPILE__)
        asm volatile("s_waitcnt vmcnt(4)" : "+v"(stg)::"memory");
#endif
        if (!__all((unsigned)(stg >> 32) == (unsigned)epoch)) {
#pragma nounroll
          for (unsigned spins = 0; spins < SPIN_CAP; ++spins) {
            stg = flag_ld(ws + WS_ST + tt * 256 + ((l16 >> 1) & 0xf8));
            if (__all((unsigned)(stg >> 32) == (unsigned)epoch)) break;
          }
        }
        mneg = -__uint_as_float((unsigned)stg << 16); pin(mneg);
        den = __uint_as_float((unsigned)stg & 0xffff0000u); pin(den);
      }
      if constexpr (hs == 76) { rg = __builtin_amdgcn_rcpf(den); pin(rg); corr_go(A0, fa0); }
      if constexpr (hs == 77) { rg *= -LOG2E; pin(rg); corr_go(A1, fa1); }
      if constexpr (hs == 80 || hs == 82) {
        if (typeB) {
          const float sc = rg * (dblA < 8 ? -0.25f : -1.0f / LOG2E);
#pragma unroll
          for (int j = 0; j < 8; j += 2) scale2(hs == 80 ? A0 : A1, j, sc);
          storeQK(dblA * 4 + 2 * ng + (hs == 82), 4);
        }
      }
      sfor<0, 24>([&](auto pc) AINL {
        constexpr int q = decltype(pc)::value, k = q / 3, ph = q % 3;
        if constexpr (hs == 78 + q) {
          if constexpr (ph == 0) epiA(A0, k);
          if constexpr (ph == 1) epiB();
          if constexpr (ph == 2) epiC(A0, k);
        }
      });
      if constexpr (hs == 102) { if (!typeB) bst16(rws, l16, WS_H + (tt * 112 + dblA * 4 + 2 * ng) * 1024, pack8(pb)); }
      if constexpr (hs == 96) corr_rd(0);
      if constexpr (hs == 98) corr1(pp);
      if constexpr (hs == 99) corr_frag(fa0);
      if constexpr (hs == 100) corr_go(B0, fa0);
      if constexpr (hs >= 103 && hs < 107) scale2(B0, 2 * (hs - 103), rg * (sngl < 8 ? -0.25f : -1.0f / LOG2E));
      if constexpr (hs == 107) { if (sngl < 16) storeQK(sngl * 4 + ng, 2); else storeV(sngl * 4 + ng, 0); }
      if constexpr (hs == 111) { if (sngl >= 16) storeV(sngl * 4 + ng, 1); }
      if constexpr (hs >= 116 && hs < 140) {
        constexpr int k = (hs - 116) / 3, ph = (hs - 116) % 3;
        if constexpr (ph == 0) epiA(A1, k);
        if constexpr (ph == 1) epiB();
        if constexpr (ph == 2) epiC(A1, k);
      }
      if constexpr (hs == FPOLL) {
        const unsigned kvo_ = bg * 128 + 1024 + (l16 & 0x100) * 4 + ((l16 & 0xf0) >> 1), qo_ = bg * 128 + qb * 16 + ((l16 & 16) >> 1);
        f1o = l16 < 512 ? kvo_ : qo_; pinu(f1o);
        f1v = flag_ld(ws + WS_F1 + f1o);
      }
      if constexpr (hs == 158) { corr_rd(0); pq = lds_ld<f32x4>(parta + 1024); }
      if constexpr (hs == 159) {
        early = __all(f1v == epoch);
        f1w = flag_ld(ws + WS_F1 + f1o);
        acquire();
        if (early) {
#pragma unroll
          for (int i = 0; i < 4; ++i) kv_first(i);
        }
      }
      if constexpr (hs == 160) corr1(pp);
      if constexpr (hs == 161) { corr2(C0); corr1(pq); }
      if constexpr (hs == 162) corr2(C1);
    });
    SB0();

    unsigned lane1 = l16; pinu(lane1); lane1 >>= 4;
    const unsigned tid1 = w * 64 + lane1;
    if (tid1 < 60) {
      const unsigned v = (tid1 & 1) ? 0u : 0x3f803f80u;
      u32x4 c = {v, v, v, v};
      lds_st<u32x4>((tid1 >> 1) * TB + 2048 + (tid1 & 1) * 16, c);
    }
    bf16x8 h0, h1;
#pragma unroll
    for (int j = 0; j < 8; ++j) {
      h0[j] = (__bf16)(C0[8 + j] * __builtin_amdgcn_rcpf(__builtin_fmaf(__builtin_amdgcn_exp2f(C0[j] * rg), den, den)));
      h1[j] = (__bf16)(C1[8 + j] * __builtin_amdgcn_rcpf(__builtin_fmaf(__builtin_amdgcn_exp2f(C1[j] * rg), den, den)));
    }
    pinb8(h0); pinb8(h1);
    SB0();
    if (!early) {
      if (!__all(f1w == epoch)) flag_wait(ws + WS_F1 + f1o, epoch);
      acquire();
#pragma unroll
      for (int i = 0; i < 4; ++i) kv_first(i);
    }
    bst16(rws, l16, WS_H + (tt * 112 + dblB * 4 + 2 * ng) * 1024, h0);
    bst16(rws, l16, WS_H + (tt * 112 + dblB * 4 + 2 * ng + 1) * 1024, h1);
    side_job(true, 0, 1);
    {
      const u32x4 sent = {~0u, ~0u, ~0u, ~0u};
      bst16(rws, l16, WS_O + ((qb * 8 + w) * 32 + head) * 1024, __builtin_bit_cast(bf16x8, sent));
    }
    wait_barrier<6, 0>();
    if (tid1 >= 256 && tid1 < 328) {
      const unsigned v = (tid1 & 1) ? 0u : 0x3f803f80u;
      u32x4 c = {v, v, v, v};
      lds_st<u32x4>(((tid1 - 196) >> 1) * TB + 2048 + (tid1 & 1) * 16, c);
    }
    hA1 = pack8(pb); hA1o = typeB ? 0u : (unsigned)WS_H + (tt * 112 + dblA * 4 + 2 * ng + 1) * 1024;
    if (w == 0) {
      const unsigned a = L1_SJ + 8192 + (4 * (lane1 >> 5) + ((lane1 & 15) >> 2)) * 64 + ((lane1 >> 4) & 1) * 32 + (lane1 & 3) * 8;
      sj9 = cat8(lds_tr(a), lds_tr(a + 512));
    }
  }

  {
    const int head = bg * 4 + (bi >> 3), qt = (bi & 7) * 8 + w;
    const int rt = bg * 4 + (bi & 3), ct = bi >> 2;
    const char* kvb = ws + WS_KV + (size_t)head * 131072 + w * 1024;
    const unsigned kvd = (w >> 1) * TB + (w & 1) * 1024;
    unsigned lane2 = l16; pinu(lane2); lane2 >>= 4;
    const int h = lane2 >> 5, d = lane2 & 31;
    const unsigned cslot = (d == 16 || d == 20) ? 2048 : 2064;
    const unsigned va0 = d < 16 ? 1024 + h * 256 + d * 16 : cslot;
    const unsigned va1 = d < 16 ? 1536 + h * 256 + d * 16 : cslot;
#define LDP(p, t) (*(const AS3 bf16x8*)((const AS3 char*)(p) + (t) * TB))
    const bf16x8 q = lds_ld<bf16x8>(L2_Q + w * 1024 + l16);
    float m = xhalf_max(fmaxf(max16(mfma(LDP(l16, 0), q, zero16())), max16(mfma(LDP(l16, 1), q, zero16()))));
    f32x16 o, negm, s;
    bf16x8 kn, v0, v1;
    f32x8 ph;
#pragma unroll
    for (int i = 0; i < 16; ++i) negm[i] = -m;
    o = zero16();
    s = mfma(LDP(l16, 0), q, negm);
    kn = LDP(l16, 1); v0 = LDP(va0, 0); v1 = v0;
#pragma unroll
    for (int i = 0; i < 8; ++i) ph[i] = 0.f;
    auto tile = [&](unsigned kp, unsigned vp0, unsigned vp1, int u) AINL {
      f32x16 sn = mfma(kn, q, negm);
      kn = LDP(kp, u + 2);
      f32x8 pl = exp8(lo8(s));
      o = pv(v1, ph, o);
      v1 = LDP(vp1, u);
      ph = exp8(hi8(s));
      o = pv(v0, pl, o);
      v0 = LDP(vp0, u + 1);
      s = sn;
    };
#define ATT_SGB() SGB(0x8, 1); SGB(0x100, 1); SGB(0x400, 5); SGB(0x8, 1); SGB(0x100, 1); SGB(0x400, 5); SGB(0x8, 1); SGB(0x100, 1)
    const unsigned hof = WS_H + (rt * 2 * 112 + w * 14) * 1024, wof = WS_WD + (ct * 2 * 112 + w * 14) * 1024;
    auto ld_frag = [&](int st, int f) AINL { return bld16(rws, l16, (f < 2 ? hof : wof) + ((f & 1) * 112 + st) * 1024); };
    constexpr int NPF = 9;
    bf16x8 fr[14][4];
    f32x16 acc[4];
    u64 f2[4];
    pin16(s); pin16(o); pin8(ph);
    wait_barrier<4, 0>();
    if (hA1o) bst16(rws, l16, hA1o, hA1);
    if (w == 0) bst16(rws, l16, WS_WD + (blockIdx.x * 9 + 8) * 1024, sj9);
    __builtin_amdgcn_s_setprio(3);
    sfor<0, 8>([&](auto ic) AINL {
      constexpr int t0 = 4 * decltype(ic)::value;
      if constexpr (t0 < 12) {
        if (w < 4) {
          if constexpr (t0 == 8) { wait_vm<9>(); lds_add(L2_F + 20, l16); }
#pragma unroll
          for (int i = t0 ? 5 + (t0 >> 2) * 9 : 6; i < 14 + (t0 >> 2) * 9; ++i) glds16s<true>(kvb + i * 4096, l16, kvd + i * 2 * TB);
        }
        if constexpr (t0 == 8) lds_wait_ge(L2_F + 20, 4);
      }
      if constexpr (t0 == 4 || t0 == 16) {
        if ((w < 4) == (t0 == 16)) {
          wait_vm<0>();
          if (lds_add(L2_F, l16) == 7 && l16 == 0) flag_st(ws + WS_F2 + blockIdx.x * 8, epoch);
        }
      }
      __builtin_amdgcn_s_setprio(att_prio(t0 >> 2));
      if constexpr (t0 == 24) lds_wait_ge(L2_F, 8);
      if constexpr (t0 == 28) {
#pragma unroll
        for (int i = 0; i < 4; ++i) f2[i] = flag_ld(ws + WS_F2 + i * 512 + (l16 >> 1));
      }
      SB0();
      const unsigned kp = l16 + t0 * TB, vp0 = va0 + t0 * TB, vp1 = va1 + t0 * TB;
#pragma unroll
      for (int u = 0; u < 4; ++u) { tile(kp, vp0, vp1, u); ATT_SGB(); SGB(0x400, 6); }
      SB0();
      pin16(o); pin16(s); pin8(ph);
    });
    lds_add(L2_F + 8, l16);
    if (!__all(f2[0] == epoch && f2[1] == epoch && f2[2] == epoch && f2[3] == epoch)) {
#pragma nounroll
      for (int i = 0; i < 4; ++i) flag_wait(ws + WS_F2 + i * 512 + (l16 >> 1), epoch);
    }
    acquire();
    sfor<0, 8>([&](auto ic) AINL {
      constexpr int it = decltype(ic)::value, t0 = 32 + 4 * it;
      __builtin_amdgcn_s_setprio(att_prio(8 + it));
      if constexpr (it == 3) lds_wait_ge(L2_F + 8, 8);
      if constexpr (it == 4) lds_add(L2_F + 16, l16);
      if constexpr (it == 6) lds_wait_ge(L2_F + 16, 8);
      if constexpr (it == 3 || it == 4) {
#pragma unroll
        for (int i = 0; i < 4; ++i) glds16s<true>(ws + WS_WO + ct * 65536 + (w * 8 + (it - 3) * 4 + i) * 1024, l16, (w * 8 + (it - 3) * 4 + i) * 1024);
      }
      if constexpr (it == 1) {
#pragma unroll
        for (int i = 0; i < 3; ++i) glds16s<true>(ws + (i < 2 ? hof : wof) + ((i & 1) * 112 + NPF + 1) * 1024, l16, L2_Q + (i * 8 + w) * 1024);
      }
      if constexpr (it == 6) {
#pragma unroll
        for (int i = 0; i < 4; ++i) glds16s<true>(ws + (i < 2 ? hof : wof) + ((i & 1) * 112 + NPF) * 1024, l16, 65536 + (w * 4 + i) * 1024);
      }
      SB0();
      const unsigned kp = l16 + t0 * TB, vp0 = va0 + t0 * TB, vp1 = va1 + t0 * TB;
#pragma unroll
      for (int u = 0; u < 4; ++u) {
        tile(kp, vp0, vp1, u);
        fr[it][u] = ld_frag(it, u);
        if (it >= 5 && u == 0) fr[8][it - 5] = ld_frag(8, it - 5);
        ATT_SGB(); SGB(0x20, 2); SGB(0x400, 6);
      }
      SB0();
      pin16(o); pin16(s); pin8(ph);
    });
    auto mm4 = [&](int st) AINL {
#pragma unroll
      for (int i = 0; i < 4; ++i) { acc[i] = mfma(fr[st][2 + (i >> 1)], fr[st][i & 1], st == 0 ? zero16() : acc[i]); pin16(acc[i]); }
    };
    auto ld4 = [&](int st) AINL {
#pragma unroll
      for (int i = 0; i < 4; ++i) fr[st][i] = ld_frag(st, i);
    };
    o = pv(v1, ph, o); pin16(o);
    SB0();
    __builtin_amdgcn_s_setprio(0);
    mm4(0); mm4(1);
    SB0();
    {
      float mx = o[8];
#pragma unroll
      for (int i = 0; i < 8; ++i) mx = fmaxf(mx, __builtin_fabsf(o[i]) * 0x1p-20f);
      bool ok = mx <= 0x1p100f && o[8] >= 0.25f;
#pragma unroll
      for (int i = 0; i <= 8; ++i) ok = ok && (o[i] == o[i]);
      if (!__all(ok)) {
        const unsigned kvo = WS_KV + head * 131072;
        float tm = m;
#pragma nounroll
        for (int t = 0; t < 64; ++t) tm = fmaxf(tm, max16(mfma(bld16(rws, l16, kvo + t * 2048), q, zero16())));
        m = xhalf_max(tm);
        o = zero16();
        const unsigned vo = d < 16 ? va0 : 0x80000000u, one = (d == 16 || d == 20) ? 0x3f803f80u : 0u;
        const u32x4 ones = {one, one, one, one};
#pragma nounroll
        for (int t = 0; t < 64; ++t) {
          s = mfma(bld16(rws, l16, kvo + t * 2048), q, zero16()) - m;
          v0 = __builtin_bit_cast(bf16x8, __builtin_bit_cast(u32x4, bld16(rws, vo, kvo + t * 2048)) | ones);
          v1 = __builtin_bit_cast(bf16x8, __builtin_bit_cast(u32x4, bld16(rws, vo, kvo + t * 2048 + 512)) | ones);
          o = pv(v0, exp8(lo8(s)), o);
          o = pv(v1, exp8(hi8(s)), o);
        }
      }
    }
    fr[8][3] = ld_frag(8, 3); fr[10][3] = ld_frag(10, 3);
    u32x4 ou;
    {
      const float inv = __builtin_amdgcn_rcpf(o[8]);
      bf16x8 ob;
#pragma unroll
      for (int j = 0; j < 8; ++j) ob[j] = (__bf16)(o[j] * inv);
      ou = __builtin_bit_cast(u32x4, ob);
#pragma unroll
      for (int i = 0; i < 4; ++i) ou[i] = ou[i] < 0xfffffffeu ? ou[i] : 0xfffffffeu;
    }
    bst16(rws, l16, WS_O + (qt * 32 + head) * 1024, __builtin_bit_cast(bf16x8, ou));
    SB0();
#pragma unroll
    for (int st = 2; st < 5; ++st) { mm4(st); SB0(); ld4(9 + st); SB0(); }
#pragma unroll
    for (int st = 5; st < 8; ++st) mm4(st);
    SB0();
    wait_vm<15>();
    lds_add(L2_F + 4, l16);
#pragma unroll
    for (int i = 0; i < 4; ++i) fr[9][i] = lds_ld<bf16x8>(65536 + (w * 4 + i) * 1024 + l16);
#pragma unroll
    for (int i = 0; i < 3; ++i) fr[10][i] = lds_ld<bf16x8>(L2_Q + (i * 8 + w) * 1024 + l16);
    mm4(8); mm4(9); mm4(10);
    SB0();
    const int mh = (w >> 1) & 1, kq = (w & 1) | ((w >> 2) << 1);
    const unsigned oof = WS_O + ((rt * 2 + mh) * 32 + kq * 8) * 1024;
    lds_wait_ge(L2_F + 4, 8);
    bf16x8 fp[16];
#pragma unroll
    for (int i = 0; i < 16; ++i) fp[i] = lds_ld<bf16x8>(((i >> 3) * 32 + kq * 8 + (i & 7)) * 1024 + l16);
    lds_add(L2_F + 12, l16);
    SB0();
    mm4(11); mm4(12); mm4(13);
    SB0();
    bf16x8 fo[8];
#pragma unroll
    for (int i = 0; i < 8; ++i) fo[i] = bld16(rws, l16, oof + i * 1024);
    unsigned lane3 = l16; pinu(lane3); lane3 >>= 4;
    const size_t eo = (size_t)((rt * 2 + mh) * 32 + (lane3 & 31)) * DM + ct * 64 + kq * 16 + 4 * (lane3 >> 5);
    const bf16x4 xv0 = *(const bf16x4*)(x + eo), xv1 = *(const bf16x4*)(x + eo + 8);
    SB0();
    lds_wait_ge(L2_F + 12, 8);
#pragma unroll
    for (int i = 0; i < 16; ++i) {
      f32x4 t = {acc[i >> 2][4 * (i & 3)], acc[i >> 2][4 * (i & 3) + 1], acc[i >> 2][4 * (i & 3) + 2], acc[i >> 2][4 * (i & 3) + 3]};
      lds_st<f32x4>(w * 16384 + i * 1024 + l16, t);
    }
    SB0();
    f32x16 c0, c1;
#pragma nounroll
    for (unsigned spins = 0;; ++spins) {
      unsigned mx = 0;
      c0 = zero16(); c1 = zero16();
#pragma unroll
      for (int i = 0; i < 8; ++i) {
        c0 = mfma(fp[i], fo[i], c0); c1 = mfma(fp[8 + i], fo[i], c1);
        const u32x4 u = __builtin_bit_cast(u32x4, fo[i]);
        mx = max(max(mx, max(u[0], u[1])), max(u[2], u[3]));
      }
      if (__all(mx != ~0u) || spins >= SPIN_CAP) break;
      asm volatile("" ::: "memory");
#pragma unroll
      for (int i = 0; i < 8; ++i) fo[i] = bld16(rws, l16, oof + i * 1024);
    }
    wait_barrier<63, 0>();
    f32x4 gp[16];
#pragma unroll
    for (int i = 0; i < 16; ++i) gp[i] = lds_ld<f32x4>((i >> 1) * 16384 + w * 2048 + (i & 1) * 1024 + l16);
    SB0();
    {
#pragma unroll
      for (int i = 0; i < 8; ++i) {
        const f32x16& c = i < 4 ? c0 : c1;
        f32x4 t = {c[4 * (i & 3)], c[4 * (i & 3) + 1], c[4 * (i & 3) + 2], c[4 * (i & 3) + 3]};
        lds_st<f32x4>((i >> 1) * 16384 + w * 2048 + (i & 1) * 1024 + l16, t);
      }
      lds_add(L2_F + 32 + mh * 4, l16);
      f32x4 g0 = ((gp[0] + gp[2]) + (gp[4] + gp[6])) + ((gp[8] + gp[10]) + (gp[12] + gp[14]));
      f32x4 g1 = ((gp[1] + gp[3]) + (gp[5] + gp[7])) + ((gp[9] + gp[11]) + (gp[13] + gp[15]));
#pragma unroll
      for (int i = 0; i < 4; ++i) {
        g0[i] = g0[i] * __builtin_amdgcn_rcpf(1.0f + __builtin_amdgcn_exp2f(g0[i] * -LOG2E));
        g1[i] = g1[i] * __builtin_amdgcn_rcpf(1.0f + __builtin_amdgcn_exp2f(g1[i] * -LOG2E));
      }
      pin4(g0); pin4(g1);
      lds_wait_ge(L2_F + 32 + mh * 4, 4);
      f32x4 rp[8];
#pragma unroll
      for (int i = 0; i < 8; ++i) rp[i] = lds_ld<f32x4>(kq * 16384 + mh * 4096 + ((i >> 1) & 1) * 2048 + (i >> 2) * 8192 + (i & 1) * 1024 + l16);
      SB0();
      const f32x4 r0 = (rp[0] + rp[2]) + (rp[4] + rp[6]), r1 = (rp[1] + rp[3]) + (rp[5] + rp[7]);
      bf16x4 o0, o1;
#pragma unroll
      for (int i = 0; i < 4; ++i) {
        o0[i] = (__bf16)__builtin_fmaf(r0[i], g0[i], (float)xv0[i]);
        o1[i] = (__bf16)__builtin_fmaf(r1[i], g1[i], (float)xv1[i]);
      }
      const u32x2 a0 = __builtin_bit_cast(u32x2, o0), a1 = __builtin_bit_cast(u32x2, o1);
      const auto sx = __builtin_amdgcn_permlane32_swap(a0[0], a1[0], false, false);
      const auto sy = __builtin_amdgcn_permlane32_swap(a0[1], a1[1], false, false);
      const u32x4 ov = {sx[0], sy[0], sx[1], sy[1]};
      __builtin_amdgcn_raw_buffer_store_b128(ov, mk_rsrc(out, SEQ * DM * 2), (unsigned)(eo + 4 * (lane3 >> 5)) * 2, 0, 0);
    }
    if (blockIdx.x == 0 && w == 0 && l16 == 0) *(u64*)(ws + WS_F) = epoch;
#if defined(__HIP_DEVICE_COMPILE__)
    asm volatile(".Lfz_end:");
#endif
  }
}

}

extern "C" void kernel_launch(void* const* d_in, const int* in_sizes, int n_in, void* d_out, int out_size,
                              void* d_ws, size_t ws_size, hipStream_t stream) {
  (void)hipFuncSetAttribute((const void*)k_fused, hipFuncAttributeMaxDynamicSharedMemorySize, LDS_BYTES);
  k_fused<<<256, 512, LDS_BYTES, stream>>>((const __bf16*)d_in[0], (const __bf16*)d_in[1], (const __bf16*)d_in[4],
                                           (const __bf16*)d_in[5], (const __bf16*)d_in[6], (const __bf16*)d_in[2],
                                           (char*)d_ws, (__bf16*)d_out);
}
```
